```python
import torch

def _softmax(x, axis=-1):
    return torch.softmax(x, dim=axis)
def _gelu(x):
    return torch.nn.functional.gelu(x, approximate="tanh")
def _silu(x):
    return torch.nn.functional.silu(x)
def _sigmoid(x):
    return torch.sigmoid(x)
def _layernorm(x, axis=-1, eps=1e-5):
    m = x.mean(dim=axis, keepdim=True)
    v = x.var(dim=axis, keepdim=True, unbiased=False)
    return (x - m) / torch.sqrt(v + eps)
def _scatter_add(tgt, idx, src, axis=0):
    return tgt.clone().index_add_(axis, idx, src)
def _segment_sum(src, seg, n):
    out = torch.zeros((n,) + src.shape[1:], dtype=src.dtype)
    return out.index_add_(0, seg, src)
def _pad(x, pw):
    flat = tuple(v for pair in reversed(pw) for v in pair)
    return torch.nn.functional.pad(x, flat)

kernel_name = 'parallel_sum__ssm_conv_s4__dense_activation_relu__prompt'

def setup_inputs():

    _w = (lambda a: (lambda t: t.to(torch.bfloat16) if t.is_floating_point() else t)(torch.as_tensor(a)))
    def _A_setup():
        B, L, D, N = 1, 2048, 512, 128
        A_re = -(lambda _x: torch.exp(torch.as_tensor(_x)))((lambda *s: torch.randn(*s, dtype=torch.bfloat16) * 3.2)(D, N))
        A_im = torch.pi * (lambda *s: torch.randn(*s, dtype=torch.bfloat16) * 0.1)(D, N)
        B_re = (lambda *s: torch.randn(*s, dtype=torch.bfloat16) * 0.4)(D, N)
        B_im = (lambda *s: torch.randn(*s, dtype=torch.bfloat16) * 0.4)(D, N)
        C_re = (lambda *s: torch.randn(*s, dtype=torch.bfloat16) * 0.4)(D, N)
        C_im = (lambda *s: torch.randn(*s, dtype=torch.bfloat16) * 0.4)(D, N)
        log_dt = (lambda *s: torch.randn(*s, dtype=torch.bfloat16) * 0.1)(D) * 0.5 - 4.0
        Dskip = (lambda *s: torch.randn(*s, dtype=torch.bfloat16) * 0.2)(D)
        return {
            "u": _w((lambda *s: torch.randn(*s, dtype=torch.bfloat16) * 0.05)(B, L, D)),
            "A_re": _w(A_re),
            "A_im": _w(A_im),
            "B_re": _w(B_re),
            "B_im": _w(B_im),
            "C_re": _w(C_re),
            "C_im": _w(C_im),
            "log_dt": _w(log_dt),
            "Dskip": _w(Dskip),
        }
    _A_in = _A_setup()

    def _B_setup():
        B, S, D, Dff = 1, 2048, 512, 1376
        return {
            "x": _w((lambda *s: torch.randn(*s, dtype=torch.bfloat16) * 0.05)(B, S, D)),
            "w1": _w((lambda *s: torch.randn(*s, dtype=torch.bfloat16) * 0.05)(D, Dff)),
            "w2": _w((lambda *s: torch.randn(*s, dtype=torch.bfloat16) * 0.05)(Dff, D)),
        }
    _B_in = _B_setup()

    return {
        "A_u": _A_in["u"],
        "A_A_re": _A_in["A_re"],
        "A_A_im": _A_in["A_im"],
        "A_B_re": _A_in["B_re"],
        "A_B_im": _A_in["B_im"],
        "A_C_re": _A_in["C_re"],
        "A_C_im": _A_in["C_im"],
        "A_log_dt": _A_in["log_dt"],
        "A_Dskip": _A_in["Dskip"],
        "B_x": _B_in["x"],
        "B_w1": _B_in["w1"],
        "B_w2": _B_in["w2"],
    }

def reference(A_u, A_A_re, A_A_im, A_B_re, A_B_im, A_C_re, A_C_im, A_log_dt, A_Dskip, B_x, B_w1, B_w2):
    def _A_ref(u, A_re, A_im, B_re, B_im, C_re, C_im, log_dt, Dskip):
        Bsz, L, D = u.shape
        dt = torch.exp(log_dt)[:, None]
        A = A_re + 1j * A_im
        Bc = B_re + 1j * B_im
        Cc = C_re + 1j * C_im
        logdA = dt * A
        dB = dt * Bc
        l = (lambda x, dt: x.to(dt))(torch.arange(L), A_re.dtype)
        P = torch.exp(logdA[:, :, None] * l[None, None, :])
        K = torch.einsum("dn,dnl,dn->dl", Cc, P, dB).real
        L2 = 2 * L
        Kp = _pad(K, ((0, 0), (0, L)))
        up = (lambda x, *d: x.permute(*d))(u, 0, 2, 1)
        up = _pad(up, ((0, 0), (0, 0), (0, L)))
        Kf = (lambda x, axes=(-2,-1): torch.fft.rfft2(x.float(), dim=axes))(Kp[:, None, :], axes=(-2, -1))
        Uf = (lambda x, axes=(-2,-1): torch.fft.rfft2(x.float(), dim=axes))(up[:, :, None, :], axes=(-2, -1))
        Yf = Uf * Kf[None, :, :, :]
        y = (lambda x, s, axes=(-2,-1): torch.fft.irfft2(x, s=s, dim=axes).to(torch.bfloat16))(Yf, (1, L2), axes=(-2, -1))[:, :, 0, :L]
        y = (lambda x, *d: x.permute(*d))(y, 0, 2, 1) + u * Dskip[None, None, :]
        return y

    def _B_ref(x, w1, w2):
        h = torch.einsum("bsd,df->bsf", x, w1)
        zero = torch.zeros_like(h)
        h = torch.maximum(h, zero)
        return torch.einsum("bsf,fd->bsd", h, w2)

    x = A_u
    xn = _layernorm(x, axis=-1)
    return x + _A_ref(xn, A_A_re, A_A_im, A_B_re, A_B_im, A_C_re, A_C_im, A_log_dt, A_Dskip) + _B_ref(xn, B_w1, B_w2)
```

```cpp
#include <hip/hip_runtime.h>
#include <hip/hip_bf16.h>
#include <stdint.h>

typedef unsigned short u16;
typedef _Float16 h8 __attribute__((ext_vector_type(8)));
typedef _Float16 h2 __attribute__((ext_vector_type(2)));
typedef __bf16 b8 __attribute__((ext_vector_type(8)));
typedef __bf16 b4 __attribute__((ext_vector_type(4)));
typedef __bf16 b2 __attribute__((ext_vector_type(2)));
typedef float f4 __attribute__((ext_vector_type(4)));
typedef float f16v __attribute__((ext_vector_type(16)));
typedef unsigned u4 __attribute__((ext_vector_type(4)));
typedef unsigned u2 __attribute__((ext_vector_type(2)));
#define LAS __attribute__((address_space(3)))
#define GAS __attribute__((address_space(1)))
#define DEVI __device__ __forceinline__

constexpr int SL = 2048, SD = 512, SN = 128, SF = 1376;
constexpr int KSTR = 2112;
constexpr size_t WS_XNT = 0;
constexpr size_t WS_KREV = WS_XNT + (size_t)SD * SL * 2;
constexpr size_t WS_KSC = WS_KREV + (size_t)SD * KSTR * 2;
constexpr size_t WS_CNT = WS_KSC + 4096;
constexpr size_t WS_H = WS_CNT + 4096;
constexpr size_t WS_AT = WS_H + (size_t)SL * SF * 2;

DEVI float bf2f(u16 v) { return __uint_as_float(((unsigned)v) << 16); }
DEVI float bfr(float f) { return (float)(__bf16)f; }
DEVI unsigned pk_bf(float a, float b) { b2 v = {(__bf16)a, (__bf16)b}; return __builtin_bit_cast(unsigned, v); }
DEVI unsigned pk_h(float a, float b) { unsigned r; asm("v_cvt_pk_f16_f32 %0, %1, %2" : "=v"(r) : "v"(a), "v"(b)); return r; }
DEVI float lo_bf(unsigned v) { return __uint_as_float(v << 16); }
DEVI float hi_bf(unsigned v) { return __uint_as_float(v & 0xffff0000u); }
template <int N> DEVI void wait_vm() { asm volatile("s_waitcnt vmcnt(%0) lgkmcnt(0)" ::"n"(N) : "memory"); }
DEVI void bar() { asm volatile("" ::: "memory"); __builtin_amdgcn_s_barrier(); asm volatile("" ::: "memory"); }
DEVI void glds16(const void* g, char* l) {
  __builtin_amdgcn_global_load_lds((const GAS void*)g, (LAS void*)l, 16, 0, 0);
}
DEVI b4 trread(const char* p) { return __builtin_amdgcn_ds_read_tr16_b64_v4bf16((LAS b4*)p); }
DEVI b8 cat(b4 a, b4 b) { return __builtin_shufflevector(a, b, 0, 1, 2, 3, 4, 5, 6, 7); }

constexpr int G1_S = 3;
constexpr int G1_STAGE = 16384;
constexpr int G1_STATS = G1_S * G1_STAGE;
constexpr int G1_LDS = G1_STATS + 512;

DEVI void g1_role(int bid, char* sm, const u16* __restrict__ x, const u16* __restrict__ w1,
                  u16* __restrict__ h, _Float16* __restrict__ xnt) {
  const int tid = threadIdx.x, l = tid & 63;
  const int w = __builtin_amdgcn_readfirstlane(tid >> 6);
  const int xcd = bid & 7, idx = bid >> 3;
  const int pm = (xcd >> 1) * 8 + (idx >> 2), pn = (xcd & 1) * 4 + (idx & 3);
  const int r0 = pm * 64, c0 = pn * 176;
  const int g = l >> 4, li = l & 15, q = li >> 2, p = l & 3;

  const int pi = tid >> 3, c8 = tid & 7;
  const u4 xa = *(const u4*)(x + (size_t)(r0 + 2 * pi) * SD + 64 * pn + 8 * c8);
  const u4 xb = *(const u4*)(x + (size_t)(r0 + 2 * pi + 1) * SD + 64 * pn + 8 * c8);

  const int xrow = 16 * w + (l >> 2);
  const u16* xsrc = x + (size_t)(r0 + xrow) * SD + (((l & 3) ^ ((xrow >> 2) & 3)) * 8);
  const u16* wsrc[3];
#pragma unroll
  for (int j = 0; j < 3; ++j) {
    int ii = 3 * w + j; if (ii > 10) ii = 9;
    const int ci = ii * 64 + l, k = ci / 22, c = ci - k * 22;
    int col = c0 + 8 * c; if (col > SF - 8) col = SF - 8;
    wsrc[j] = w1 + (size_t)k * SF + col;
  }
  auto issue = [&](int kk, int slot) {
    char* sb = sm + slot * G1_STAGE;
    glds16(xsrc + kk * 32, sb + w * 1024);
#pragma unroll
    for (int j = 0; j < 3; ++j) glds16(wsrc[j] + (size_t)kk * 32 * SF, sb + 4096 + (3 * w + j) * 1024);
  };
  int xoff[4][2];
#pragma unroll
  for (int s = 0; s < 4; ++s)
#pragma unroll
    for (int t = 0; t < 2; ++t)
      xoff[s][t] = ((w + s) & 3) * 1024 + li * 64 + (((2 * t + (g >> 1)) ^ q) * 16) + 8 * (g & 1);
  int woff[3];
#pragma unroll
  for (int j = 0; j < 3; ++j) {
    int nt = 3 * w + j; if (nt > 10) nt = 10;
    woff[j] = 4096 + (4 * g + q) * 352 + 32 * nt + 8 * p;
  }

  f4 acc[4][3];
#pragma unroll
  for (int s = 0; s < 4; ++s)
#pragma unroll
    for (int j = 0; j < 3; ++j) acc[s][j] = (f4){0.f, 0.f, 0.f, 0.f};
  float rsum = 0.f, rsq = 0.f, cs[3] = {0.f, 0.f, 0.f};
  const b2 ones = {(__bf16)1.0f, (__bf16)1.0f};

#pragma unroll
  for (int s = 0; s < G1_S - 1; ++s) issue(s, s);

#pragma unroll
  for (int kk = 0; kk < 16; ++kk) {
    constexpr int dummy = 0; (void)dummy;
    const int later = ((kk + G1_S - 1) < 16 ? (kk + G1_S - 1) : 16) - kk - 1;
    if (later >= 2) wait_vm<8>(); else if (later == 1) wait_vm<4>(); else wait_vm<0>();
    bar();
    if (kk + G1_S - 1 < 16) issue(kk + G1_S - 1, (kk + G1_S - 1) % G1_S);
    const char* sb = sm + (kk % G1_S) * G1_STAGE;
    b8 xf[4], wf[3];
#pragma unroll
    for (int s = 0; s < 4; ++s) xf[s] = cat(*(const b4*)(sb + xoff[s][0]), *(const b4*)(sb + xoff[s][1]));
#pragma unroll
    for (int j = 0; j < 3; ++j) wf[j] = cat(trread(sb + woff[j]), trread(sb + woff[j] + 16 * 352));
#pragma unroll
    for (int s = 0; s < 4; ++s)
#pragma unroll
      for (int j = 0; j < 3; ++j) acc[s][j] = __builtin_amdgcn_mfma_f32_16x16x32_bf16(wf[j], xf[s], acc[s][j], 0, 0, 0);
#pragma unroll
    for (int e = 0; e < 4; ++e) {
      const b2 v = {xf[0][2 * e], xf[0][2 * e + 1]};
      rsum = __builtin_amdgcn_fdot2_f32_bf16(v, ones, rsum, false);
      rsq = __builtin_amdgcn_fdot2_f32_bf16(v, v, rsq, false);
#pragma unroll
      for (int j = 0; j < 3; ++j) {
        const b2 u = {wf[j][2 * e], wf[j][2 * e + 1]};
        cs[j] = __builtin_amdgcn_fdot2_f32_bf16(u, ones, cs[j], false);
      }
    }
  }
  rsum += __shfl_xor(rsum, 16); rsum += __shfl_xor(rsum, 32);
  rsq += __shfl_xor(rsq, 16); rsq += __shfl_xor(rsq, 32);
#pragma unroll
  for (int j = 0; j < 3; ++j) { cs[j] += __shfl_xor(cs[j], 16); cs[j] += __shfl_xor(cs[j], 32); }
  float2* stats = (float2*)(sm + G1_STATS);
  {
    const float mean = rsum * (1.0f / SD);
    float var = rsq * (1.0f / SD) - mean * mean; var = var < 0.f ? 0.f : var;
    const float mb = bfr(mean), vb = bfr(var);
    const float sb = bfr(sqrtf(bfr(vb + 1e-5f)));
    if (l < 16) stats[16 * w + l] = make_float2(mb, sb);
  }
  __syncthreads();
  float csr[3][4];
#pragma unroll
  for (int j = 0; j < 3; ++j)
#pragma unroll
    for (int r = 0; r < 4; ++r) csr[j][r] = __shfl(cs[j], 4 * g + r);
#pragma unroll
  for (int s = 0; s < 4; ++s) {
    const int row = 16 * ((w + s) & 3) + li;
    const float2 st = stats[row];
    const float inv = 1.0f / st.y;
#pragma unroll
    for (int j = 0; j < 3; ++j) {
      const int nt = 3 * w + j, col = c0 + 16 * nt + 4 * g;
      float v[4];
#pragma unroll
      for (int r = 0; r < 4; ++r) { v[r] = bfr((acc[s][j][r] - st.x * csr[j][r]) * inv); v[r] = v[r] > 0.f ? v[r] : 0.f; }
      if (nt < 11 && col < SF) {
        u2 o = {pk_bf(v[0], v[1]), pk_bf(v[2], v[3])};
        *(u2*)(h + (size_t)(r0 + row) * SF + col) = o;
      }
    }
  }
  {
    const float2 s0 = stats[2 * pi], s1 = stats[2 * pi + 1];
    unsigned* T = (unsigned*)sm;
#pragma unroll
    for (int e = 0; e < 8; ++e) {
      const unsigned wa = xa[e >> 1], wb = xb[e >> 1];
      const float a = (e & 1) ? hi_bf(wa) : lo_bf(wa), b = (e & 1) ? hi_bf(wb) : lo_bf(wb);
      const float na = bfr(bfr(a - s0.x) / s0.y), nb = bfr(bfr(b - s1.x) / s1.y);
      T[(8 * c8 + e) * 32 + (pi ^ (c8 << 2))] = pk_h(na, nb);
    }
    __syncthreads();
#pragma unroll
    for (int rr = 0; rr < 2; ++rr) {
      const int ch = (tid >> 3) + 32 * rr, m = tid & 7;
      const u4 v = *(const u4*)(T + ch * 32 + 4 * (m ^ (ch >> 3)));
      *(u4*)(xnt + (size_t)(64 * pn + ch) * SL + r0 + 8 * m) = v;
    }
  }
}

constexpr int KG_ROW = 528;
constexpr int KG_Y = 0, KG_X = 40 * KG_ROW, KG_KC = KG_X + 41 * KG_ROW;
constexpr int KG_LDS = KG_KC + 656 * 2 + 16;

struct cf { float r, i; };
DEVI cf cmul(cf a, cf b) { return {a.r * b.r - a.i * b.i, a.r * b.i + a.i * b.r}; }
DEVI cf cseed(float al, float om, float m) {
  const float mag = __builtin_amdgcn_exp2f((al * m) * 1.44269504088896341f);
  const float ph = om * m;
  const float chi = 0.15915494f, clo = 6.4206382e-9f;
  const float fr = __builtin_fmaf(ph, clo, __builtin_fmaf(ph, chi, -__builtin_rintf(ph * chi)));
  return {mag * __builtin_amdgcn_cosf(fr), mag * __builtin_amdgcn_sinf(fr)};
}

DEVI void kg_role(int d, char* sm, const u16* __restrict__ Are, const u16* __restrict__ Aim,
                  const u16* __restrict__ Bre, const u16* __restrict__ Bim, const u16* __restrict__ Cre,
                  const u16* __restrict__ Cim, const u16* __restrict__ logdt, _Float16* __restrict__ krev,
                  float* __restrict__ ksc) {
  const int tid = threadIdx.x, l = tid & 63;
  const int w = __builtin_amdgcn_readfirstlane(tid >> 6);
  const int n = tid & 127;
  const float dt = bfr(expf(bf2f(logdt[d])));
  const float al = dt * bf2f(Are[d * SN + n]), om = dt * bf2f(Aim[d * SN + n]);
  if (w < 2) {
    char* Y = sm + KG_Y + 4 * n;
    cf P[16], Q[8], R[8];
    const cf e1 = cseed(al, om, 1.f), e16 = cseed(al, om, 16.f), e32 = cseed(al, om, 32.f), e64 = cseed(al, om, 64.f);
    P[0] = {1.f, 0.f}; P[1] = e1;
#pragma unroll
    for (int j = 2; j < 16; ++j) P[j] = cmul(P[j >> 1], P[j - (j >> 1)]);
#pragma unroll
    for (int j = 0; j < 16; ++j) *(unsigned*)(Y + j * KG_ROW) = pk_h(P[j].r, P[j].i);
#pragma unroll
    for (int m = 0; m < 8; ++m) { Q[m] = cmul(e16, P[2 * m]); *(unsigned*)(Y + (16 + m) * KG_ROW) = pk_h(Q[m].r, Q[m].i); }
#pragma unroll
    for (int m = 0; m < 8; ++m) { R[m] = cmul(e32, m < 4 ? P[4 * m] : Q[2 * (m - 4)]); *(unsigned*)(Y + (24 + m) * KG_ROW) = pk_h(R[m].r, R[m].i); }
#pragma unroll
    for (int m = 0; m < 8; ++m) {
      const cf t = cmul(e64, m < 2 ? P[8 * m] : (m < 4 ? Q[4 * (m - 2)] : R[2 * (m - 4)]));
      *(unsigned*)(Y + (32 + m) * KG_ROW) = pk_h(t.r, t.i);
    }
  } else {
    cf cb[2];
    float mx = 0.f;
#pragma unroll
    for (int hh = 0; hh < 2; ++hh) {
      const int nn = (n & 63) + 64 * hh;
      const float br = dt * bf2f(Bre[d * SN + nn]), bi = dt * bf2f(Bim[d * SN + nn]);
      const float cr = bf2f(Cre[d * SN + nn]), ci = bf2f(Cim[d * SN + nn]);
      cb[hh] = cmul({cr, ci}, {br, bi});
      mx = fmaxf(mx, fmaxf(fabsf(cb[hh].r), fabsf(cb[hh].i)));
    }
#pragma unroll
    for (int o = 32; o >= 1; o >>= 1) mx = fmaxf(mx, __shfl_xor(mx, o));
    int ex = (mx > 0.f && mx < 3.0e38f) ? __builtin_amdgcn_frexp_expf(mx) : 0;
    ex = ex < -100 ? -100 : (ex > 100 ? 100 : ex);
    const float sc = __builtin_amdgcn_ldexpf(1.0f, -ex);
    if (tid == 128) ksc[d] = __builtin_amdgcn_ldexpf(1.0f, ex);
    const cf c0 = (w == 2) ? cb[0] : cb[1];
    const cf cbs = {c0.r * sc, c0.i * sc};
    char* X = sm + KG_X + 4 * n;
    const cf e16 = cseed(al, om, 16.f), e32 = cseed(al, om, 32.f), e64 = cseed(al, om, 64.f), e128 = cseed(al, om, 128.f);
    const cf e256 = cseed(al, om, 256.f), e512 = cseed(al, om, 512.f), e1024 = cseed(al, om, 1024.f);
    cf a0 = cbs, a0b = cmul(cbs, e128), a1 = cmul(cbs, e256), a2 = cmul(cbs, e512), a3 = cmul(cbs, e1024);
#pragma unroll
    for (int i = 0; i < 8; ++i) {
      *(unsigned*)(X + i * KG_ROW) = pk_h(a0.r, -a0.i);
      *(unsigned*)(X + (8 + i) * KG_ROW) = pk_h(a0b.r, -a0b.i);
      *(unsigned*)(X + (16 + i) * KG_ROW) = pk_h(a1.r, -a1.i);
      *(unsigned*)(X + (24 + i) * KG_ROW) = pk_h(a2.r, -a2.i);
      *(unsigned*)(X + (32 + i) * KG_ROW) = pk_h(a3.r, -a3.i);
      a0 = cmul(a0, e16); a0b = cmul(a0b, e16); a1 = cmul(a1, e32); a2 = cmul(a2, e64); a3 = cmul(a3, e128);
    }
    *(unsigned*)(X + 40 * KG_ROW) = pk_h(a3.r, -a3.i);
  }
  __syncthreads();
  {
    const int li = l & 15, g = l >> 4;
    int xr, yr;
    if (w == 0) { xr = li; yr = li; }
    else if (w == 1) { xr = 16 + li; yr = li < 8 ? 2 * li : 8 + li; }
    else if (w == 2) { xr = 16 + li; yr = li < 4 ? 4 * li : (li < 8 ? 8 + 2 * li : 16 + li); }
    else { xr = 32 + (li < 9 ? li : 8); yr = li < 2 ? 8 * li : (li < 4 ? 8 + 4 * li : (li < 8 ? 16 + 2 * li : 24 + li)); }
    const char* xa = sm + KG_X + xr * KG_ROW + 16 * g;
    const char* ya = sm + KG_Y + yr * KG_ROW + 16 * g;
    f4 acc = {0.f, 0.f, 0.f, 0.f};
#pragma unroll
    for (int kk = 0; kk < 8; ++kk)
      acc = __builtin_amdgcn_mfma_f32_16x16x32_f16(*(const h8*)(xa + 64 * kk), *(const h8*)(ya + 64 * kk), acc, 0, 0, 0);
    _Float16* Kc = (_Float16*)(sm + KG_KC);
#pragma unroll
    for (int r = 0; r < 4; ++r) {
      const int i = 4 * g + r;
      int idx = -1;
      if (w == 0) idx = 16 * i + li;
      else if (w == 1) { if (i < 8) idx = 256 + 16 * i + li; }
      else if (w == 2) { if (i >= 8) idx = 384 + 16 * (i - 8) + li; }
      else { if (i < 9) idx = 512 + 16 * i + li; }
      if (idx >= 0) Kc[idx] = (_Float16)acc[r];
    }
  }
  __syncthreads();
  {
    const _Float16* Kc = (const _Float16*)(sm + KG_KC);
    h8 o;
#pragma unroll
    for (int e = 0; e < 8; ++e) {
      const int lg = 2047 - (8 * tid + e);
      const int sh = lg < 256 ? 0 : (lg < 512 ? 1 : (lg < 1024 ? 2 : 3));
      const int r = sh == 0 ? lg : ((lg + (1 << (sh - 1)) - 1 + ((lg >> sh) & 1)) >> sh);
      o[e] = Kc[128 * sh + r];
    }
    *(h8*)(krev + (size_t)d * KSTR + 8 * tid) = o;
    if (tid < 8) *(u4*)(krev + (size_t)d * KSTR + 2048 + 8 * tid) = (u4){0u, 0u, 0u, 0u};
  }
}

constexpr int CV_KC = 4160;
constexpr int CV_U = 8 * CV_KC + 512;
constexpr int CV_LDS = CV_U + 95 * 80 + 16;
constexpr int CV_RS = 144;

DEVI int cv_copy_base(int s) { return s * CV_KC; }

DEVI void cv_role(int d, char* sm, const _Float16* __restrict__ xnt, const _Float16* __restrict__ krev,
                  const float* __restrict__ ksc, const u16* __restrict__ dskip, u16* __restrict__ at,
                  unsigned* __restrict__ cnt) {
  const int tid = threadIdx.x, l = tid & 63;
  const int w = __builtin_amdgcn_readfirstlane(tid >> 6);
  const u4 uv = *(const u4*)(xnt + (size_t)d * SL + 8 * tid);
  const u4* kg = (const u4*)(krev + (size_t)d * KSTR);
  const u4 k0 = kg[tid], k1 = kg[tid + 1];
  u4 k2 = {0u, 0u, 0u, 0u}, k3 = {0u, 0u, 0u, 0u};
  if (tid < 3) { k2 = kg[256 + tid]; k3 = kg[257 + tid]; }
  const float scale = ksc[d];
  const float dsk = bf2f(dskip[d]);
  *(u4*)(sm + CV_U + (31 + (tid >> 2)) * 80 + (tid & 3) * 16) = uv;
  if (tid < 155) *(u4*)(sm + CV_U + tid * 16) = (u4){0u, 0u, 0u, 0u};
  auto build = [&](u4 a, u4 b, int q) {
    const unsigned wv[8] = {a.x, a.y, a.z, a.w, b.x, b.y, b.z, b.w};
    unsigned f[7];
#pragma unroll
    for (int m = 0; m < 7; ++m) f[m] = __builtin_amdgcn_alignbit(wv[m + 1], wv[m], 16);
#pragma unroll
    for (int m = 0; m < 4; ++m) {
      *(u4*)(sm + cv_copy_base(2 * m) + 16 * q) = (u4){wv[m], wv[m + 1], wv[m + 2], wv[m + 3]};
      *(u4*)(sm + cv_copy_base(2 * m + 1) + 16 * q) = (u4){f[m], f[m + 1], f[m + 2], f[m + 3]};
    }
  };
  build(k0, k1, tid);
  if (tid < 3) build(k2, k3, 256 + tid);
  __syncthreads();
  const int ks = w & 1, hb = w >> 1;
  const int b = l & 31, hh = l >> 5;
  const char* abase = sm + cv_copy_base(7 - (b & 7)) + 16 * (255 - 4 * (16 * hb + 47) - (b >> 3) + 2 * ks + hh);
  const char* bbase = sm + CV_U + (31 - 16 * hb - 15 + b) * 80 + 32 * ks + 16 * hh;
  f16v t0 = {}, t1 = {};
#pragma unroll
  for (int i = 0; i < 16; ++i) {
    const h8 a_lo = *(const h8*)(abase + 64 * (15 - i) + 2048);
    const h8 a_hi = *(const h8*)(abase + 64 * (15 - i));
    const h8 b_lo = *(const h8*)(bbase + 80 * (15 - i));
    const h8 b_hi = *(const h8*)(bbase + 80 * (15 - i) + 2560);
    t0 = __builtin_amdgcn_mfma_f32_32x32x16_f16(a_lo, b_lo, t0, 0, 0, 0);
    t1 = __builtin_amdgcn_mfma_f32_32x32x16_f16(a_lo, b_hi, t1, 0, 0, 0);
    t1 = __builtin_amdgcn_mfma_f32_32x32x16_f16(a_hi, b_lo, t1, 0, 0, 0);
  }
  __syncthreads();
  {
    char* rb = sm + w * (64 * CV_RS);
#pragma unroll
    for (int r4 = 0; r4 < 4; ++r4) {
      const int b0 = 8 * r4 + 4 * hh;
      *(f4*)(rb + b * CV_RS + 4 * b0) = (f4){t0[4 * r4], t0[4 * r4 + 1], t0[4 * r4 + 2], t0[4 * r4 + 3]};
      *(f4*)(rb + (32 + b) * CV_RS + 4 * b0) = (f4){t1[4 * r4], t1[4 * r4 + 1], t1[4 * r4 + 2], t1[4 * r4 + 3]};
    }
  }
  __syncthreads();
  {
    const int a = tid >> 2, b0 = (tid & 3) * 8;
    f4 s0 = {0.f, 0.f, 0.f, 0.f}, s1 = {0.f, 0.f, 0.f, 0.f};
#pragma unroll
    for (int ww = 0; ww < 4; ++ww) {
      const char* rb = sm + ww * (64 * CV_RS) + a * CV_RS + 4 * b0;
      s0 += *(const f4*)rb; s1 += *(const f4*)(rb + 16);
    }
    const h8 uu = __builtin_bit_cast(h8, uv);
    float o[8];
#pragma unroll
    for (int e = 0; e < 8; ++e) {
      const float y = bfr((e < 4 ? s0[e] : s1[e - 4]) * scale);
      o[e] = y + bfr((float)uu[e] * dsk);
    }
    const u4 ov = {pk_bf(o[0], o[1]), pk_bf(o[2], o[3]), pk_bf(o[4], o[5]), pk_bf(o[6], o[7])};
    const __amdgpu_buffer_rsrc_t rs = __builtin_amdgcn_make_buffer_rsrc(at, 0, SD * SL * 2, 0x00020000);
    __builtin_amdgcn_raw_buffer_store_b128(ov, rs, (d * SL + 8 * tid) * 2, 0, 16);
  }
  asm volatile("s_waitcnt vmcnt(0)" ::: "memory");
  __syncthreads();
  if (tid == 0) __hip_atomic_fetch_add((GAS unsigned*)(cnt + (d >> 6) * 64), 1u, __ATOMIC_RELAXED, __HIP_MEMORY_SCOPE_AGENT);
}

constexpr int G2_S = 6;
constexpr int G2_STAGE = 8192;
constexpr int G2_LDS = G2_S * G2_STAGE;
constexpr int G2_NK = SF / 32;

DEVI void g2_role(int bid, char* sm, const u16* __restrict__ x, const u16* __restrict__ h,
                  const u16* __restrict__ w2, const u16* __restrict__ at, unsigned* __restrict__ cnt,
                  u16* __restrict__ out) {
  const int tid = threadIdx.x, l = tid & 63;
  const int w = __builtin_amdgcn_readfirstlane(tid >> 6);
  const int xcd = bid & 7, idx = bid >> 3;
  const int pm = (xcd >> 1) * 8 + (idx >> 2), pn = (xcd & 1) * 4 + (idx & 3);
  const int t0 = pm * 64, d0 = pn * 64;
  const int g = l >> 4, li = l & 15, q = li >> 2, p = l & 3;
  const int wt = w >> 1, wd = w & 1;

  u2 xr[2][2];
#pragma unroll
  for (int dj = 0; dj < 2; ++dj)
#pragma unroll
    for (int ti = 0; ti < 2; ++ti)
      xr[dj][ti] = *(const u2*)(x + (size_t)(t0 + 16 * (2 * wt + ti) + li) * SD + d0 + 16 * (2 * wd + dj) + 4 * g);

  const int hrow = 16 * w + (l >> 2);
  const u16* hsrc = h + (size_t)(t0 + hrow) * SF + (((l & 3) ^ ((4 - ((hrow >> 2) & 3)) & 3)) * 8);
  const int krow = 8 * w + (l >> 3);
  const int f2w = ((krow >> 1) & 1) | (((krow >> 3) & 1) << 1);
  const u16* wsrc = w2 + (size_t)krow * SD + d0 + (((l & 7) ^ (f2w << 1)) * 8);
  auto issue = [&](int kk, int slot) {
    char* sb = sm + slot * G2_STAGE;
    glds16(hsrc + kk * 32, sb + w * 1024);
    glds16(wsrc + (size_t)kk * 32 * SD, sb + 4096 + w * 1024);
  };
  int hoff[2], woff[2][2];
#pragma unroll
  for (int ti = 0; ti < 2; ++ti) hoff[ti] = (16 * (2 * wt + ti) + li) * 64 + ((g ^ ((4 - q) & 3)) * 16);
#pragma unroll
  for (int dj = 0; dj < 2; ++dj)
#pragma unroll
    for (int t = 0; t < 2; ++t) {
      const int row = 8 * g + 4 * t + q;
      const int f2 = ((row >> 1) & 1) | (((row >> 3) & 1) << 1);
      woff[dj][t] = 4096 + row * 128 + (((2 * (2 * wd + dj) + (p >> 1)) ^ (f2 << 1)) * 16) + 8 * (p & 1);
    }
  f4 acc[2][2];
#pragma unroll
  for (int dj = 0; dj < 2; ++dj)
#pragma unroll
    for (int ti = 0; ti < 2; ++ti) acc[dj][ti] = (f4){0.f, 0.f, 0.f, 0.f};

  auto compute = [&](int slot) {
    const char* sb = sm + slot * G2_STAGE;
    b8 hf[2], wf[2];
#pragma unroll
    for (int ti = 0; ti < 2; ++ti) hf[ti] = *(const b8*)(sb + hoff[ti]);
#pragma unroll
    for (int dj = 0; dj < 2; ++dj) wf[dj] = cat(trread(sb + woff[dj][0]), trread(sb + woff[dj][1]));
#pragma unroll
    for (int dj = 0; dj < 2; ++dj)
#pragma unroll
      for (int ti = 0; ti < 2; ++ti) acc[dj][ti] = __builtin_amdgcn_mfma_f32_16x16x32_bf16(wf[dj], hf[ti], acc[dj][ti], 0, 0, 0);
  };

#pragma unroll
  for (int s = 0; s < G2_S - 1; ++s) issue(s, s);
  int slot = 0, islot = G2_S - 1;
  for (int kk = 0; kk < G2_NK - (G2_S - 1); ++kk) {
    wait_vm<2 * (G2_S - 2)>();
    bar();
    issue(kk + G2_S - 1, islot);
    compute(slot);
    slot = slot == G2_S - 1 ? 0 : slot + 1;
    islot = islot == G2_S - 1 ? 0 : islot + 1;
  }
  if (w == 0) {
    GAS unsigned* c = (GAS unsigned*)(cnt + pn * 64);
    for (unsigned spins = 0; spins < (1u << 22); ++spins) {
      const unsigned v = __builtin_amdgcn_readfirstlane(__hip_atomic_load(c, __ATOMIC_RELAXED, __HIP_MEMORY_SCOPE_AGENT));
      if (v >= 64u) break;
      __builtin_amdgcn_s_sleep(2);
    }
  }
  u4 av[2];
  const __amdgpu_buffer_rsrc_t rs = __builtin_amdgcn_make_buffer_rsrc((void*)at, 0, SD * SL * 2, 0x00020000);
#pragma unroll
  for (int tk = 0; tk < G2_S - 1; ++tk) {
    if (tk == 0) wait_vm<2 * (G2_S - 2)>();
    else if (tk == 1) wait_vm<2 * (G2_S - 3) + 2>();
    else if (tk == 2) wait_vm<2 * (G2_S - 4) + 2>();
    else if (tk == 3) wait_vm<2 * (G2_S - 5) + 2>();
    else wait_vm<2>();
    bar();
    if (tk == 0) {
#pragma unroll
      for (int rr = 0; rr < 2; ++rr)
        av[rr] = __builtin_amdgcn_raw_buffer_load_b128(rs, ((d0 + (tid >> 3) + 32 * rr) * SL + t0 + 8 * (tid & 7)) * 2, 0, 16);
    }
    compute(slot);
    slot = slot == G2_S - 1 ? 0 : slot + 1;
  }
  __syncthreads();
#pragma unroll
  for (int rr = 0; rr < 2; ++rr) *(u4*)(sm + ((tid >> 3) + 32 * rr) * 128 + 16 * (tid & 7)) = av[rr];
  __syncthreads();
#pragma unroll
  for (int dj = 0; dj < 2; ++dj)
#pragma unroll
    for (int ti = 0; ti < 2; ++ti) {
      const int dt_ = 2 * wd + dj, tt_ = 2 * wt + ti;
      const b4 a4 = trread(sm + (16 * dt_ + 4 * g + q) * 128 + (16 * tt_ + 4 * p) * 2);
      const u2 xv = xr[dj][ti];
      const float xs[4] = {lo_bf(xv.x), hi_bf(xv.x), lo_bf(xv.y), hi_bf(xv.y)};
      float o[4];
#pragma unroll
      for (int r = 0; r < 4; ++r) o[r] = bfr(xs[r] + (float)a4[r]) + bfr(acc[dj][ti][r]);
      const u2 ov = {pk_bf(o[0], o[1]), pk_bf(o[2], o[3])};
      *(u2*)(out + (size_t)(t0 + 16 * tt_ + li) * SD + d0 + 16 * dt_ + 4 * g) = ov;
    }
}

constexpr int P1_LDS = G1_LDS > KG_LDS ? G1_LDS : KG_LDS;
constexpr int P2_LDS = G2_LDS > CV_LDS ? G2_LDS : CV_LDS;

__global__ __launch_bounds__(256, 3) void k_phase1(const u16* x, const u16* Are, const u16* Aim, const u16* Bre,
                                                   const u16* Bim, const u16* Cre, const u16* Cim, const u16* logdt,
                                                   const u16* w1, char* ws) {
  extern __shared__ __attribute__((aligned(16))) char sm[];
  const int bid = blockIdx.x;
  if (bid < 256) {
    if (bid == 0 && threadIdx.x < 8) ((unsigned*)(ws + WS_CNT))[threadIdx.x * 64] = 0u;
    g1_role(bid, sm, x, w1, (u16*)(ws + WS_H), (_Float16*)(ws + WS_XNT));
  } else {
    kg_role(bid - 256, sm, Are, Aim, Bre, Bim, Cre, Cim, logdt, (_Float16*)(ws + WS_KREV), (float*)(ws + WS_KSC));
  }
}

__global__ __launch_bounds__(256, 3) void k_phase2(const u16* x, const u16* dskip, const u16* w2, char* ws, u16* out) {
  extern __shared__ __attribute__((aligned(16))) char sm[];
  const int bid = blockIdx.x;
  if (bid < 256) {
    g2_role(bid, sm, x, (const u16*)(ws + WS_H), w2, (const u16*)(ws + WS_AT), (unsigned*)(ws + WS_CNT), out);
  } else {
    cv_role(bid - 256, sm, (const _Float16*)(ws + WS_XNT), (const _Float16*)(ws + WS_KREV), (const float*)(ws + WS_KSC),
            dskip, (u16*)(ws + WS_AT), (unsigned*)(ws + WS_CNT));
  }
}

extern "C" void kernel_launch(void* const* d_in, const int* in_sizes, int n_in, void* d_out, int out_size,
                              void* d_ws, size_t ws_size, hipStream_t stream) {
  const u16* x = (const u16*)d_in[0];
  k_phase1<<<768, 256, P1_LDS, stream>>>(x, (const u16*)d_in[1], (const u16*)d_in[2], (const u16*)d_in[3],
                                         (const u16*)d_in[4], (const u16*)d_in[5], (const u16*)d_in[6],
                                         (const u16*)d_in[7], (const u16*)d_in[10], (char*)d_ws);
  k_phase2<<<768, 256, P2_LDS, stream>>>(x, (const u16*)d_in[8], (const u16*)d_in[11], (char*)d_ws, (u16*)d_out);
}
```

```cpp
#include <hip/hip_runtime.h>
#include <hip/hip_bf16.h>
#include <stdint.h>

typedef unsigned short u16;
typedef _Float16 h8 __attribute__((ext_vector_type(8)));
typedef __bf16 b8 __attribute__((ext_vector_type(8)));
typedef __bf16 b4 __attribute__((ext_vector_type(4)));
typedef __bf16 b2 __attribute__((ext_vector_type(2)));
typedef float f4 __attribute__((ext_vector_type(4)));
typedef float f16v __attribute__((ext_vector_type(16)));
typedef unsigned u4 __attribute__((ext_vector_type(4)));
typedef unsigned u2 __attribute__((ext_vector_type(2)));
#define LAS __attribute__((address_space(3)))
#define GAS __attribute__((address_space(1)))
#define DEVI __device__ __forceinline__

constexpr int SL = 2048, SD = 512, SN = 128, SF = 1376;
constexpr int KSTR = 2112;
constexpr int NKS = SF / 32;
constexpr size_t WS_XNT = 0;
constexpr size_t WS_KREV = WS_XNT + (size_t)SD * SL * 2;
constexpr size_t WS_KSC = WS_KREV + (size_t)SD * KSTR * 2;
constexpr size_t WS_CNT = WS_KSC + 4096;
constexpr size_t WS_H = WS_CNT + 4096;
constexpr size_t WS_AT = WS_H + (size_t)SL * SF * 2;
constexpr size_t WS_W2F = WS_AT + (size_t)SD * SL * 2;

#define STAMP(ws, slot) do {} while (0)
DEVI float bf2f(u16 v) { return __uint_as_float(((unsigned)v) << 16); }
DEVI float bfr(float f) { return (float)(__bf16)f; }
DEVI unsigned pk_bf(float a, float b) { b2 v = {(__bf16)a, (__bf16)b}; return __builtin_bit_cast(unsigned, v); }
DEVI unsigned pk_h(float a, float b) { unsigned r; asm("v_cvt_pk_f16_f32 %0, %1, %2" : "=v"(r) : "v"(a), "v"(b)); return r; }
DEVI float lo_bf(unsigned v) { return __uint_as_float(v << 16); }
DEVI float hi_bf(unsigned v) { return __uint_as_float(v & 0xffff0000u); }
template <int N> DEVI void wait_vm() { asm volatile("s_waitcnt vmcnt(%0) lgkmcnt(0)" ::"n"(N) : "memory"); }
DEVI void bar() { asm volatile("" ::: "memory"); __builtin_amdgcn_s_barrier(); asm volatile("" ::: "memory"); }
DEVI void lbar() { asm volatile("s_waitcnt lgkmcnt(0)" ::: "memory"); __builtin_amdgcn_s_barrier(); asm volatile("" ::: "memory"); }
DEVI void glds16(const void* g, char* l) {
  __builtin_amdgcn_global_load_lds((const GAS void*)g, (LAS void*)l, 16, 0, 0);
}
DEVI b4 trread(const char* p) { return __builtin_amdgcn_ds_read_tr16_b64_v4bf16((LAS b4*)p); }
DEVI b8 cat(b4 a, b4 b) { return __builtin_shufflevector(a, b, 0, 1, 2, 3, 4, 5, 6, 7); }
template <int V> struct IC { static constexpr int v = V; };
template <int I, int N, class F> DEVI void sfor(F&& f) { if constexpr (I < N) { f(IC<I>{}); sfor<I + 1, N>(f); } }
#define PIN(x) asm volatile("" : "+v"(x))
__device__ const unsigned g_ones[4] = {0x3f803f80u, 0x3f803f80u, 0x3f803f80u, 0x3f803f80u};

constexpr int G1_S = 5;
constexpr int G1_STAGE = 16384;
constexpr int G1_LDS = G1_S * G1_STAGE;

template <int WM>
DEVI void g1_body(int bid, char* sm, const u16* __restrict__ x, const u16* __restrict__ w1,
                  u16* __restrict__ h, _Float16* __restrict__ xnt, int w) {
  const int tid = threadIdx.x, l = tid & 63;
  const int wn = w & 3;
  const int xcd = bid & 7, idx = bid >> 3;
  const int pm = (xcd >> 1) * 8 + (idx >> 2), pn = (xcd & 1) * 4 + (idx & 3);
  const int r0 = pm * 64, c0 = pn * 176;
  const int g = l >> 4, li = l & 15, q = li >> 2, p = l & 3;

  const char* ga[2];
  long gs[2];
#pragma unroll
  for (int j = 0; j < 2; ++j) {
    const int pc = 2 * w + j;
    if (pc < 4) {
      const int xrow = 16 * pc + (l >> 2);
      ga[j] = (const char*)(x + (size_t)(r0 + xrow) * SD + (((l & 3) ^ ((xrow >> 2) & 3)) * 8));
      gs[j] = 64;
    } else {
      const int ci = (pc - 4) * 64 + l, k = ci / 24, cp = ci - k * 24;
      const int c = cp ^ (((k >> 1) & 3) << 1);
      int col = c0 + 8 * c; if (col > SF - 8) col = SF - 8;
      ga[j] = c >= 22 ? (const char*)g_ones : (const char*)(w1 + (size_t)k * SF + col);
      gs[j] = c >= 22 ? 0 : 32 * SF * 2;
    }
  }
  char* const lb = sm + w * 2048;
  auto issue = [&](int slot, int j) { glds16(ga[j], lb + slot * G1_STAGE + j * 1024); ga[j] += gs[j]; };
#pragma unroll
  for (int s = 0; s < G1_S; ++s) { issue(s, 0); issue(s, 1); }

  const int mt0 = 2 * WM + (wn & 1), mt1 = 2 * WM + ((wn & 1) ^ 1);
  const int xo0 = li * 64 + (((g >> 1) ^ q) * 16) + 8 * (g & 1), xo1 = li * 64 + (((2 + (g >> 1)) ^ q) * 16) + 8 * (g & 1);
  const char* xa00 = sm + mt0 * 1024 + xo0; const char* xa01 = sm + mt0 * 1024 + xo1;
  const char* xa10 = sm + mt1 * 1024 + xo0; const char* xa11 = sm + mt1 * 1024 + xo1;
  const char* wa[3];
#pragma unroll
  for (int j = 0; j < 3; ++j) {
    const int row = 4 * g + q;
    wa[j] = sm + 4096 + row * 384 + (((3 * wn + j) ^ ((row >> 1) & 3)) * 32) + 8 * p;
  }
  const int pi = tid >> 4, c4 = tid & 15;
  unsigned hoff[3];
#pragma unroll
  for (int j = 0; j < 3; ++j) {
    const int col = c0 + 16 * (3 * wn + j) + 4 * g;
    const bool ok = (3 * wn + j < 11) && col < SF;
    hoff[j] = ok ? (unsigned)(((pm * 4 * NKS + (col >> 5)) * 64 + ((col >> 3) & 3) * 16 + li) * 16 + (col & 7) * 2) : 0x80000000u;
    PIN(hoff[j]);
  }
  u2 xva, xvb;

  f4 acc[2][3], cs[3], rq = {0.f, 0.f, 0.f, 0.f};
#pragma unroll
  for (int j = 0; j < 3; ++j) { acc[0][j] = acc[1][j] = cs[j] = (f4){0.f, 0.f, 0.f, 0.f}; }
  const b8 ones = {(__bf16)1.0f, (__bf16)1.0f, (__bf16)1.0f, (__bf16)1.0f, (__bf16)1.0f, (__bf16)1.0f, (__bf16)1.0f, (__bf16)1.0f};
  b8 xf[2][2], wf[2][3];
  auto rd = [&](int buf, int so) {
#pragma unroll
    for (int j = 0; j < 3; ++j) wf[buf][j] = cat(trread(wa[j] + so), trread(wa[j] + so + 16 * 384));
    xf[buf][0] = cat(*(const b4*)(xa00 + so), *(const b4*)(xa01 + so));
    xf[buf][1] = cat(*(const b4*)(xa10 + so), *(const b4*)(xa11 + so));
  };
  wait_vm<2 * (G1_S - 1)>();
  bar();
  rd(0, 0);
  sfor<0, 16>([&](auto K) {
    constexpr int k = K.v, u = k & 1;
    if constexpr (k < 15) {
      constexpr int later = ((k + G1_S) < 16 ? (k + G1_S) : 16) - (k + 2);
      wait_vm<2 * later + (k > 11 ? 2 : 0)>();
      bar();
      if constexpr (k + G1_S < 16) issue(k % G1_S, 0);
      if constexpr (k == 11) {
        xva = *(const u2*)(x + (size_t)(r0 + 2 * pi) * SD + 64 * pn + 4 * c4);
        xvb = *(const u2*)(x + (size_t)(r0 + 2 * pi + 1) * SD + 64 * pn + 4 * c4);
      }
      rd(u ^ 1, ((k + 1) % G1_S) * G1_STAGE);
    }
    __builtin_amdgcn_sched_barrier(0);
#pragma unroll
    for (int j = 0; j < 3; ++j) acc[0][j] = __builtin_amdgcn_mfma_f32_16x16x32_bf16(wf[u][j], xf[u][0], acc[0][j], 0, 0, 0);
    rq = __builtin_amdgcn_mfma_f32_16x16x32_bf16(xf[u][0], xf[u][0], rq, 0, 0, 0);
    __builtin_amdgcn_sched_barrier(0);
    if constexpr (k + G1_S < 16) issue(k % G1_S, 1);
    __builtin_amdgcn_sched_barrier(0);
#pragma unroll
    for (int j = 0; j < 3; ++j) acc[1][j] = __builtin_amdgcn_mfma_f32_16x16x32_bf16(wf[u][j], xf[u][1], acc[1][j], 0, 0, 0);
    if constexpr (u == WM) {
#pragma unroll
      for (int j = 0; j < 3; ++j) cs[j] = __builtin_amdgcn_mfma_f32_16x16x32_bf16(wf[u][j], ones, cs[j], 0, 0, 0);
    }
    __builtin_amdgcn_sched_barrier(0);
  });
  STAMP(xnt, 4);
  bar();
  float* raw = (float*)(sm + 8192);
  float2* stats = (float2*)(sm + 9216);
  float* csum = (float*)(sm + 10240);
  if (wn == 3 && g == 0) { raw[16 * mt0 + li] = acc[0][2][0]; raw[16 * mt1 + li] = acc[1][2][0]; }
  if (wn < 2 && g == q) raw[64 + 16 * mt0 + li] = p == 0 ? rq[0] : (p == 1 ? rq[1] : (p == 2 ? rq[2] : rq[3]));
  if (li == 0) {
#pragma unroll
    for (int j = 0; j < 3; ++j) *(f4*)(csum + WM * 192 + 16 * (3 * wn + j) + 4 * g) = cs[j];
  }
  lbar();
  if (tid < 64) {
    const float mean = raw[tid] * (1.0f / SD);
    float var = raw[64 + tid] * (1.0f / SD) - mean * mean; var = var < 0.f ? 0.f : var;
    stats[tid] = make_float2(bfr(mean), bfr(sqrtf(bfr(bfr(var) + 1e-5f))));
  }
  f4 csr[3];
#pragma unroll
  for (int j = 0; j < 3; ++j)
    csr[j] = *(const f4*)(csum + 16 * (3 * wn + j) + 4 * g) + *(const f4*)(csum + 192 + 16 * (3 * wn + j) + 4 * g);
  lbar();
  const __amdgpu_buffer_rsrc_t rh = __builtin_amdgcn_make_buffer_rsrc((void*)h, 0, SL * SF * 2, 0x00020000);
#pragma unroll
  for (int s = 0; s < 2; ++s) {
    const int mt = s ? mt1 : mt0;
    const float2 st = stats[16 * mt + li];
    const float inv = __builtin_amdgcn_rcpf(st.y);
#pragma unroll
    for (int j = 0; j < 3; ++j) {
      float v[4];
#pragma unroll
      for (int r = 0; r < 4; ++r) v[r] = fmaxf((acc[s][j][r] - st.x * csr[j][r]) * inv, 0.f);
      const u2 o = {pk_bf(v[0], v[1]), pk_bf(v[2], v[3])};
      __builtin_amdgcn_raw_buffer_store_b64(o, rh, hoff[j], mt * (NKS * 1024), 0);
    }
  }
  {
    const float2 s0 = stats[2 * pi], s1 = stats[2 * pi + 1];
    unsigned* T = (unsigned*)sm;
#pragma unroll
    for (int e = 0; e < 4; ++e) {
      const unsigned wva = xva[e >> 1], wvb = xvb[e >> 1];
      const float a = (e & 1) ? hi_bf(wva) : lo_bf(wva), b = (e & 1) ? hi_bf(wvb) : lo_bf(wvb);
      const float na = bfr(bfr(a - s0.x) / s0.y), nb = bfr(bfr(b - s1.x) / s1.y);
      T[(4 * c4 + e) * 32 + (pi ^ ((c4 >> 1) << 2))] = pk_h(na, nb);
    }
    lbar();
    const int ch = tid >> 3, m = tid & 7;
    const u4 v = *(const u4*)(T + ch * 32 + 4 * (m ^ (ch >> 3)));
    *(u4*)(xnt + (size_t)(64 * pn + ch) * SL + r0 + 8 * m) = v;
  }
}

DEVI void g1_role(int bid, char* sm, const u16* __restrict__ x, const u16* __restrict__ w1,
                  u16* __restrict__ h, _Float16* __restrict__ xnt) {
  const int w = __builtin_amdgcn_readfirstlane(threadIdx.x >> 6);
  if (w < 4) g1_body<0>(bid, sm, x, w1, h, xnt, w); else g1_body<1>(bid, sm, x, w1, h, xnt, w);
}

constexpr int KG_CH = 80 * 512;
constexpr int KG_LDS = 2 * KG_CH;

struct cf { float r, i; };
DEVI cf cmul(cf a, cf b) {
  float t, u, re, im;
  asm("v_mul_f32 %0, %2, %3\n\tv_mul_f32 %1, %2, %4" : "=&v"(t), "=&v"(u) : "v"(a.i), "v"(b.i), "v"(b.r));
  asm("v_fma_f32 %0, %2, %3, -%4\n\tv_fma_f32 %1, %2, %5, %6" : "=&v"(re), "=&v"(im) : "v"(a.r), "v"(b.r), "v"(t), "v"(b.i), "v"(u));
  return {re, im};
}
DEVI cf cseed(float al, float om, float m) {
  const float mag = __builtin_amdgcn_exp2f((al * m) * 1.44269504088896341f);
  const float ph = om * m;
  const float chi = 0.15915494f, clo = 6.4206382e-9f;
  const float fr = __builtin_fmaf(ph, clo, __builtin_fmaf(ph, chi, -__builtin_rintf(ph * chi)));
  return {mag * __builtin_amdgcn_cosf(fr), mag * __builtin_amdgcn_sinf(fr)};
}

DEVI void kg_role(int kb, char* sm0, const u16* __restrict__ Are, const u16* __restrict__ Aim,
                  const u16* __restrict__ Bre, const u16* __restrict__ Bim, const u16* __restrict__ Cre,
                  const u16* __restrict__ Cim, const u16* __restrict__ logdt, const u16* __restrict__ w2,
                  _Float16* __restrict__ krev, float* __restrict__ ksc, u16* __restrict__ w2f) {
  const int l = threadIdx.x & 63;
  const int w8 = __builtin_amdgcn_readfirstlane(threadIdx.x >> 6);
  const int w = w8 & 3, tid = threadIdx.x & 255;
  const int d = 2 * kb + (w8 >> 2);
  char* sm = sm0 + (w8 >> 2) * KG_CH;
  const int n = tid & 127;
  const u16 ldt = logdt[d], are = Are[d * SN + n], aim = Aim[d * SN + n];
  const int fr = kb * 8 + w8;
  u16 wv[8] = {0, 0, 0, 0, 0, 0, 0, 0};
  if (fr < NKS * 32) {
    const u16* src = w2 + (size_t)(32 * (fr >> 5) + 8 * (l >> 4)) * SD + 16 * (fr & 31) + (l & 15);
#pragma unroll
    for (int e = 0; e < 8; ++e) wv[e] = src[(size_t)e * SD];
  }
  int xr, yr;
  {
    const int li = l & 15, y = li << w;
    yr = y < 16 ? y : (y < 32 ? 8 + (y >> 1) : (y < 64 ? 16 + (y >> 2) : 24 + (y >> 3)));
    xr = 40 + (w == 0 ? li : (w == 3 ? 32 + (li & 7) : 16 + li));
  }
  const int kbase = w == 0 ? 0 : (w == 3 ? 512 : 256), ilo = w == 2 ? 8 : 0, ihi = (w & 1) ? 8 : 16;
  const float dt = bfr(expf(bf2f(ldt)));
  const float al = dt * bf2f(are), om = dt * bf2f(aim);
  const int wa = (n >> 2) * 16 + (n & 3) * 4;
#define KG_ST(row, re, im) *(unsigned*)(sm + (row) * 512 + (wa ^ (((row) & 15) << 4))) = pk_h(re, im)
  float k2048 = 0.f;
  if (w < 2) {
    cf P[16], Q[8], R[8];
    const cf e1 = cseed(al, om, 1.f), e16 = cseed(al, om, 16.f), e32 = cseed(al, om, 32.f), e64 = cseed(al, om, 64.f);
    P[0] = {1.f, 0.f}; P[1] = e1;
#pragma unroll
    for (int j = 2; j < 16; ++j) P[j] = cmul(P[j >> 1], P[j - (j >> 1)]);
#pragma unroll
    for (int j = 0; j < 16; ++j) KG_ST(j, P[j].r, P[j].i);
    Q[0] = e16;
#pragma unroll
    for (int m = 1; m < 8; ++m) Q[m] = cmul(e16, P[2 * m]);
#pragma unroll
    for (int m = 0; m < 8; ++m) KG_ST(16 + m, Q[m].r, Q[m].i);
    R[0] = e32;
#pragma unroll
    for (int m = 1; m < 8; ++m) R[m] = cmul(e32, m < 4 ? P[4 * m] : Q[2 * (m - 4)]);
#pragma unroll
    for (int m = 0; m < 8; ++m) KG_ST(24 + m, R[m].r, R[m].i);
    KG_ST(32, e64.r, e64.i);
#pragma unroll
    for (int m = 1; m < 8; ++m) {
      const cf t = cmul(e64, m < 2 ? P[8 * m] : (m < 4 ? Q[4 * (m - 2)] : R[2 * (m - 4)]));
      KG_ST(32 + m, t.r, t.i);
    }
  } else {
    cf cb[2];
    float mx = 0.f;
#pragma unroll
    for (int hh = 0; hh < 2; ++hh) {
      const int nn = (n & 63) + 64 * hh;
      const float br = dt * bf2f(Bre[d * SN + nn]), bi = dt * bf2f(Bim[d * SN + nn]);
      const float cr = bf2f(Cre[d * SN + nn]), ci = bf2f(Cim[d * SN + nn]);
      cb[hh] = cmul({cr, ci}, {br, bi});
      mx = fmaxf(mx, fmaxf(fabsf(cb[hh].r), fabsf(cb[hh].i)));
    }
    const cf e16 = cseed(al, om, 16.f), e32 = cseed(al, om, 32.f), e64 = cseed(al, om, 64.f), e128 = cseed(al, om, 128.f);
    const cf e256 = cseed(al, om, 256.f), e512 = cseed(al, om, 512.f), e1024 = cseed(al, om, 1024.f);
#pragma unroll
    for (int o = 32; o >= 1; o >>= 1) mx = fmaxf(mx, __shfl_xor(mx, o));
    int ex = (mx > 0.f && mx < 3.0e38f) ? __builtin_amdgcn_frexp_expf(mx) : 0;
    ex = ex < -100 ? -100 : (ex > 100 ? 100 : ex);
    const float sc = __builtin_amdgcn_ldexpf(1.0f, -ex);
    if (tid == 128) ksc[d] = __builtin_amdgcn_ldexpf(1.0f, ex);
    const cf c0 = (w == 2) ? cb[0] : cb[1];
    const cf cbs = {c0.r * sc, c0.i * sc};
    cf a0 = cbs, a0b = cmul(cbs, e128), a1 = cmul(cbs, e256), a2 = cmul(cbs, e512), a3 = cmul(cbs, e1024);
#pragma unroll
    for (int i = 0; i < 8; ++i) {
      KG_ST(40 + i, a0.r, -a0.i);
      KG_ST(48 + i, a0b.r, -a0b.i);
      KG_ST(56 + i, a1.r, -a1.i);
      KG_ST(64 + i, a2.r, -a2.i);
      KG_ST(72 + i, a3.r, -a3.i);
      if (i < 7) { a0 = cmul(a0, e16); a0b = cmul(a0b, e16); a1 = cmul(a1, e32); a2 = cmul(a2, e64); }
      a3 = cmul(a3, e128);
    }
    k2048 = a3.r;
#pragma unroll
    for (int o = 32; o >= 1; o >>= 1) k2048 += __shfl_xor(k2048, o);
  }
#undef KG_ST
  if (fr < (NKS + 1) * 32) {
    const u4 f = {(unsigned)wv[0] | ((unsigned)wv[1] << 16), (unsigned)wv[2] | ((unsigned)wv[3] << 16),
                  (unsigned)wv[4] | ((unsigned)wv[5] << 16), (unsigned)wv[6] | ((unsigned)wv[7] << 16)};
    *(u4*)(w2f + ((size_t)fr * 64 + l) * 8) = f;
  }
  STAMP((char*)krev - WS_KREV, 4);
  lbar();
  STAMP((char*)krev - WS_KREV, 5);
  {
    const int g = l >> 4;
    const char* xa = sm + xr * 512;
    const char* ya = sm + yr * 512;
    h8 xf[8], yf[8];
#pragma unroll
    for (int kk = 0; kk < 8; ++kk) {
      xf[kk] = *(const h8*)(xa + (((4 * kk + g) ^ (xr & 15)) * 16));
      yf[kk] = *(const h8*)(ya + (((4 * kk + g) ^ (yr & 15)) * 16));
    }
    lbar();
    f4 acc0 = {0.f, 0.f, 0.f, 0.f}, acc1 = {0.f, 0.f, 0.f, 0.f};
#pragma unroll
    for (int kk = 0; kk < 4; ++kk) {
      acc0 = __builtin_amdgcn_mfma_f32_16x16x32_f16(xf[2 * kk], yf[2 * kk], acc0, 0, 0, 0);
      acc1 = __builtin_amdgcn_mfma_f32_16x16x32_f16(xf[2 * kk + 1], yf[2 * kk + 1], acc1, 0, 0, 0);
    }
    _Float16* Kc = (_Float16*)sm;
#pragma unroll
    for (int r = 0; r < 4; ++r) {
      const int i = 4 * g + r;
      const bool ok = (i >= ilo) && (i < ihi);
      Kc[ok ? kbase + 16 * i + (l & 15) : 648] = (_Float16)(acc0[r] + acc1[r]);
    }
    if (w >= 2 && l == 0) ((float*)(sm + 1536))[w - 2] = k2048;
  }
  lbar();
  STAMP((char*)krev - WS_KREV, 6);
  {
    const _Float16* Kc = (const _Float16*)sm;
    const float* kp = (const float*)(sm + 1536);
    const _Float16 klast = (_Float16)(kp[0] + kp[1]);
    h8 o;
#pragma unroll
    for (int e = 0; e < 8; ++e) {
      const int lg = 2047 - (8 * tid + e);
      const int sh = lg < 256 ? 0 : (lg < 512 ? 1 : (lg < 1024 ? 2 : 3));
      const int r = sh == 0 ? lg : ((lg + (1 << (sh - 1)) - 1 + ((lg >> sh) & 1)) >> sh);
      const int ix = 128 * sh + r;
      const _Float16 v = Kc[ix < 640 ? ix : 0];
      o[e] = ix < 640 ? v : klast;
    }
    *(h8*)(krev + (size_t)d * KSTR + 8 * tid) = o;
    if (tid < 8) *(u4*)(krev + (size_t)d * KSTR + 2048 + 8 * tid) = (u4){0u, 0u, 0u, 0u};
  }
}

constexpr int CV_KC = 4160;
constexpr int CV_U = 8 * CV_KC;
constexpr int CV_CH = CV_U + 95 * 80;
constexpr int CV_LDS = 2 * CV_CH;
constexpr int CV_RS = 144;

DEVI void cv_role(int cb, char* sm0, const _Float16* __restrict__ xnt, const _Float16* __restrict__ krev,
                  const float* __restrict__ ksc, const u16* __restrict__ dskip, u16* __restrict__ at,
                  unsigned* __restrict__ cnt) {
  __builtin_amdgcn_s_setprio(2);
  const int l = threadIdx.x & 63;
  const int w8 = __builtin_amdgcn_readfirstlane(threadIdx.x >> 6);
  const int tid = threadIdx.x & 255, w = w8 & 3;
  const int d = 2 * cb + (w8 >> 2);
  char* sm = sm0 + (w8 >> 2) * CV_CH;
  const u4 uv = *(const u4*)(xnt + (size_t)d * SL + 8 * tid);
  const u4* kg = (const u4*)(krev + (size_t)d * KSTR);
  const u4 k0 = kg[tid], k1 = kg[tid + 1];
  u4 k2 = {0u, 0u, 0u, 0u}, k3 = {0u, 0u, 0u, 0u};
  if (tid < 3) { k2 = kg[256 + tid]; k3 = kg[257 + tid]; }
  const float scale = ksc[d];
  const float dsk = bf2f(dskip[d]);
  if (tid < 155) *(u4*)(sm + CV_U + tid * 16) = (u4){0u, 0u, 0u, 0u};
  *(u4*)(sm + CV_U + (31 + (tid >> 2)) * 80 + (tid & 3) * 16) = uv;
  auto build = [&](u4 a, u4 b, int q) {
    const unsigned wv[8] = {a.x, a.y, a.z, a.w, b.x, b.y, b.z, b.w};
    unsigned f[7];
#pragma unroll
    for (int m = 0; m < 7; ++m) f[m] = __builtin_amdgcn_alignbit(wv[m + 1], wv[m], 16);
#pragma unroll
    for (int m = 0; m < 4; ++m) {
      *(u4*)(sm + (2 * m) * CV_KC + 16 * q) = (u4){wv[m], wv[m + 1], wv[m + 2], wv[m + 3]};
      *(u4*)(sm + (2 * m + 1) * CV_KC + 16 * q) = (u4){f[m], f[m + 1], f[m + 2], f[m + 3]};
    }
  };
  build(k0, k1, tid);
  if (tid < 3) build(k2, k3, 256 + tid);
  lbar();
  STAMP(xnt, 8);
  const int ks = w & 1, hb = w >> 1;
  const int b = l & 31, hh = l >> 5;
  const char* abase = sm + (7 - (b & 7)) * CV_KC + 16 * (255 - 4 * (16 * hb + 47) - (b >> 3) + 2 * ks + hh);
  const char* bbase = sm + CV_U + (31 - 16 * hb - 15 + b) * 80 + 32 * ks + 16 * hh;
  f16v t0 = {}, t1 = {};
#pragma unroll
  for (int i = 0; i < 16; ++i) {
    const h8 a_lo = *(const h8*)(abase + 64 * (15 - i) + 2048);
    const h8 a_hi = *(const h8*)(abase + 64 * (15 - i));
    const h8 b_lo = *(const h8*)(bbase + 80 * (15 - i));
    const h8 b_hi = *(const h8*)(bbase + 80 * (15 - i) + 2560);
    t0 = __builtin_amdgcn_mfma_f32_32x32x16_f16(a_lo, b_lo, t0, 0, 0, 0);
    t1 = __builtin_amdgcn_mfma_f32_32x32x16_f16(a_lo, b_hi, t1, 0, 0, 0);
    t1 = __builtin_amdgcn_mfma_f32_32x32x16_f16(a_hi, b_lo, t1, 0, 0, 0);
  }
  lbar();
  STAMP(xnt, 9);
  {
    char* rb = sm + w * (64 * CV_RS) + b * CV_RS + 16 * hh;
#pragma unroll
    for (int r4 = 0; r4 < 4; ++r4) {
      *(f4*)(rb + 32 * r4) = (f4){t0[4 * r4], t0[4 * r4 + 1], t0[4 * r4 + 2], t0[4 * r4 + 3]};
      *(f4*)(rb + 32 * CV_RS + 32 * r4) = (f4){t1[4 * r4], t1[4 * r4 + 1], t1[4 * r4 + 2], t1[4 * r4 + 3]};
    }
  }
  lbar();
  {
    const char* rb = sm + (tid >> 2) * CV_RS + (tid & 3) * 32;
    f4 s0 = *(const f4*)rb, s1 = *(const f4*)(rb + 16);
#pragma unroll
    for (int ww = 1; ww < 4; ++ww) { s0 += *(const f4*)(rb + ww * (64 * CV_RS)); s1 += *(const f4*)(rb + ww * (64 * CV_RS) + 16); }
    const h8 uu = __builtin_bit_cast(h8, uv);
    float o[8];
#pragma unroll
    for (int e = 0; e < 8; ++e) o[e] = bfr((e < 4 ? s0[e] : s1[e - 4]) * scale) + bfr((float)uu[e] * dsk);
    const u4 ov = {pk_bf(o[0], o[1]), pk_bf(o[2], o[3]), pk_bf(o[4], o[5]), pk_bf(o[6], o[7])};
    const __amdgpu_buffer_rsrc_t rs = __builtin_amdgcn_make_buffer_rsrc(at, 0, SD * SL * 2, 0x00020000);
    __builtin_amdgcn_raw_buffer_store_b128(ov, rs, (d * SL + 8 * tid) * 2, 0, 16);
  }
  STAMP(xnt, 10);
  asm volatile("s_waitcnt vmcnt(0)" ::: "memory");
  __syncthreads();
  STAMP(xnt, 11);
  if (threadIdx.x == 0) __hip_atomic_fetch_add((GAS unsigned*)(cnt + (cb >> 5) * 64), 1u, __ATOMIC_RELAXED, __HIP_MEMORY_SCOPE_AGENT);
}

constexpr int G2_S = 5;
constexpr int G2_STAGE = 16384;
constexpr int G2_LDS = G2_S * G2_STAGE;
constexpr int G2_NST = 22;

DEVI void g2_role(int bid, char* sm, const u16* __restrict__ x, const u16* __restrict__ hfr,
                  const u16* __restrict__ w2f, const u16* __restrict__ at, unsigned* __restrict__ cnt,
                  u16* __restrict__ out) {
  const int tid = threadIdx.x, l = tid & 63;
  const int w = __builtin_amdgcn_readfirstlane(tid >> 6);
  const int xcd = bid & 7, idx = bid >> 3;
  const int pm = (xcd >> 1) * 8 + (idx >> 2), pn = (xcd & 1) * 4 + (idx & 3);
  const int t0 = pm * 64, d0 = pn * 64;
  const int g = l >> 4, li = l & 15, q = li >> 2, p = l & 3;
  const int wt = w >> 1, wd = w & 1;
  const char* ga = w < 4 ? (const char*)hfr + ((size_t)(pm * 4 + w) * NKS * 64 + l) * 16
                         : (const char*)w2f + ((size_t)(pn * 4 + w - 4) * 64 + l) * 16;
  const long gs = w < 4 ? 1024 : 32768;
  char* const lb = sm + w * 1024;
  auto issue = [&](int slot, int j) { glds16(ga, lb + slot * G2_STAGE + j * 8192); ga += gs; };
#pragma unroll
  for (int s = 0; s < G2_S; ++s) { issue(s, 0); issue(s, 1); }
  u2 xr[2];
#pragma unroll
  for (int j = 0; j < 2; ++j) xr[j] = *(const u2*)(x + (size_t)(t0 + 16 * wt + li) * SD + d0 + 16 * (2 * wd + j) + 4 * g);
  const char* ha = sm + wt * 1024 + l * 16;
  const char* wa = sm + 4096 + (2 * wd) * 1024 + l * 16;
  f4 acc[2] = {{0.f, 0.f, 0.f, 0.f}, {0.f, 0.f, 0.f, 0.f}};
  b8 hf[2][2], wf[2][2][2];
  auto rd = [&](int buf, int so) {
#pragma unroll
    for (int j = 0; j < 2; ++j) {
      hf[buf][j] = *(const b8*)(ha + so + j * 8192);
      wf[buf][j][0] = *(const b8*)(wa + so + j * 8192);
      wf[buf][j][1] = *(const b8*)(wa + so + j * 8192 + 1024);
    }
  };
  u4 av;
  const __amdgpu_buffer_rsrc_t rs = __builtin_amdgcn_make_buffer_rsrc((void*)at, 0, SD * SL * 2, 0x00020000);
  wait_vm<2 * (G2_S - 1) + 2>();
  bar();
  rd(0, 0);
  sfor<0, G2_NST>([&](auto K) {
    constexpr int k = K.v, u = k & 1;
    constexpr int KP = G2_NST - G2_S;
    if constexpr (k < G2_NST - 1) {
      constexpr int later = ((k + G2_S) < G2_NST ? (k + G2_S) : G2_NST) - (k + 2);
      wait_vm<2 * later + (k < G2_S - 1 ? 2 : 0) + (k > KP ? 1 : 0)>();
      bar();
      if constexpr (k + G2_S < G2_NST) {
        issue(k % G2_S, 0);
      }
      rd(u ^ 1, ((k + 1) % G2_S) * G2_STAGE);
    }
    __builtin_amdgcn_sched_barrier(0);
    acc[0] = __builtin_amdgcn_mfma_f32_16x16x32_bf16(wf[u][0][0], hf[u][0], acc[0], 0, 0, 0);
    acc[1] = __builtin_amdgcn_mfma_f32_16x16x32_bf16(wf[u][0][1], hf[u][0], acc[1], 0, 0, 0);
    __builtin_amdgcn_sched_barrier(0);
    if constexpr (k + G2_S < G2_NST) {
      if constexpr (k + G2_S == G2_NST - 1) { if (w < 4) ga -= 1024; }
      issue(k % G2_S, 1);
    }
    if constexpr (k == KP) {
      STAMP((char*)hfr - WS_H, 8);
      GAS unsigned* c = (GAS unsigned*)(cnt + pn * 64);
      for (unsigned spins = 0; spins < (1u << 22); ++spins) {
        const unsigned v = __builtin_amdgcn_readfirstlane(__hip_atomic_load(c, __ATOMIC_RELAXED, __HIP_MEMORY_SCOPE_AGENT));
        if (v >= 32u) break;
        __builtin_amdgcn_s_sleep(1);
      }
      asm volatile("" ::: "memory");
      STAMP((char*)hfr - WS_H, 9);
      av = __builtin_amdgcn_raw_buffer_load_b128(rs, ((d0 + 32 * wd + (l >> 1)) * SL + t0 + 16 * wt + 8 * (l & 1)) * 2, 0, 16);
    }
    __builtin_amdgcn_sched_barrier(0);
    acc[0] = __builtin_amdgcn_mfma_f32_16x16x32_bf16(wf[u][1][0], hf[u][1], acc[0], 0, 0, 0);
    acc[1] = __builtin_amdgcn_mfma_f32_16x16x32_bf16(wf[u][1][1], hf[u][1], acc[1], 0, 0, 0);
    __builtin_amdgcn_sched_barrier(0);
  });
  STAMP((char*)hfr - WS_H, 10);
  char* myA = sm + 3 * G2_STAGE + w * 1024;
  *(u4*)(myA + l * 16) = av;
  asm volatile("s_waitcnt lgkmcnt(0)" ::: "memory");
#pragma unroll
  for (int j = 0; j < 2; ++j) {
    const b4 a4 = trread(myA + (16 * j + 4 * g + q) * 32 + 8 * p);
    const u2 xv = xr[j];
    const float xs[4] = {lo_bf(xv.x), hi_bf(xv.x), lo_bf(xv.y), hi_bf(xv.y)};
    float o[4];
#pragma unroll
    for (int r = 0; r < 4; ++r) o[r] = bfr(xs[r] + (float)a4[r]) + bfr(acc[j][r]);
    const u2 ov = {pk_bf(o[0], o[1]), pk_bf(o[2], o[3])};
    *(u2*)(out + (size_t)(t0 + 16 * wt + li) * SD + d0 + 16 * (2 * wd + j) + 4 * g) = ov;
  }
}

constexpr int P1_LDS = G1_LDS > KG_LDS ? G1_LDS : KG_LDS;
constexpr int P2_LDS = G2_LDS > CV_LDS ? G2_LDS : CV_LDS;

__global__ __launch_bounds__(512, 4) void k_phase1(const u16* x, const u16* Are, const u16* Aim, const u16* Bre,
                                                   const u16* Bim, const u16* Cre, const u16* Cim, const u16* logdt,
                                                   const u16* w1, const u16* w2, char* ws, int boff) {
  extern __shared__ __attribute__((aligned(16))) char sm[];
  const int bid = blockIdx.x + boff;
  STAMP(ws, 0);
  if (bid < 256) {
    if (bid == 0 && threadIdx.x < 8) ((unsigned*)(ws + WS_CNT))[threadIdx.x * 64] = 0u;
    g1_role(bid, sm, x, w1, (u16*)(ws + WS_H), (_Float16*)(ws + WS_XNT));
  } else {
    kg_role(bid - 256, sm, Are, Aim, Bre, Bim, Cre, Cim, logdt, w2, (_Float16*)(ws + WS_KREV), (float*)(ws + WS_KSC),
            (u16*)(ws + WS_W2F));
  }
  STAMP(ws, 1);
}

__global__ __launch_bounds__(512, 4) void k_phase2(const u16* x, const u16* dskip, char* ws, u16* out, int boff) {
  extern __shared__ __attribute__((aligned(16))) char sm[];
  const int bid = blockIdx.x + boff;
  STAMP(ws, 2);
  if (bid < 256) {
    g2_role(bid, sm, x, (const u16*)(ws + WS_H), (const u16*)(ws + WS_W2F), (const u16*)(ws + WS_AT),
            (unsigned*)(ws + WS_CNT), out);
  } else {
    cv_role(bid - 256, sm, (const _Float16*)(ws + WS_XNT), (const _Float16*)(ws + WS_KREV), (const float*)(ws + WS_KSC),
            dskip, (u16*)(ws + WS_AT), (unsigned*)(ws + WS_CNT));
  }
  STAMP(ws, 3);
}

static void launch_p1(void* const* d_in, void* d_ws, hipStream_t stream, int grid, int boff) {
  (void)hipFuncSetAttribute((const void*)k_phase1, hipFuncAttributeMaxDynamicSharedMemorySize, P1_LDS);
  k_phase1<<<grid, 512, P1_LDS, stream>>>((const u16*)d_in[0], (const u16*)d_in[1], (const u16*)d_in[2], (const u16*)d_in[3],
                                          (const u16*)d_in[4], (const u16*)d_in[5], (const u16*)d_in[6],
                                          (const u16*)d_in[7], (const u16*)d_in[10], (const u16*)d_in[11], (char*)d_ws, boff);
}
static void launch_p2(void* const* d_in, void* d_out, void* d_ws, hipStream_t stream, int grid, int boff) {
  (void)hipFuncSetAttribute((const void*)k_phase2, hipFuncAttributeMaxDynamicSharedMemorySize, P2_LDS);
  k_phase2<<<grid, 512, P2_LDS, stream>>>((const u16*)d_in[0], (const u16*)d_in[8], (char*)d_ws, (u16*)d_out, boff);
}

extern "C" void kernel_launch(void* const* d_in, const int* in_sizes, int n_in, void* d_out, int out_size,
                              void* d_ws, size_t ws_size, hipStream_t stream) {
  launch_p1(d_in, d_ws, stream, 512, 0);
  launch_p2(d_in, d_out, d_ws, stream, 512, 0);
}
```

```cpp
#include <hip/hip_runtime.h>
#include <hip/hip_bf16.h>
#include <stdint.h>

typedef unsigned short u16;
typedef _Float16 h8 __attribute__((ext_vector_type(8)));
typedef __bf16 b8 __attribute__((ext_vector_type(8)));
typedef __bf16 b4 __attribute__((ext_vector_type(4)));
typedef __bf16 b2 __attribute__((ext_vector_type(2)));
typedef float f4 __attribute__((ext_vector_type(4)));
typedef float f16v __attribute__((ext_vector_type(16)));
typedef unsigned u4 __attribute__((ext_vector_type(4)));
typedef unsigned u2 __attribute__((ext_vector_type(2)));
#define LAS __attribute__((address_space(3)))
#define GAS __attribute__((address_space(1)))
#define DEVI __device__ __forceinline__

constexpr int SL = 2048, SD = 512, SN = 128, SF = 1376;
constexpr int KSTR = 2112;
constexpr int NKS = SF / 32;
constexpr int NKP = NKS + 1;
constexpr size_t WS_XNT = 0;
constexpr size_t WS_KREV = WS_XNT + (size_t)SD * SL * 2;
constexpr size_t WS_KSC = WS_KREV + (size_t)SD * KSTR * 2;
constexpr int CNT_STR = 1088;
constexpr size_t WS_CNT = WS_KSC + 4096;
constexpr size_t WS_H = WS_CNT + 64 * CNT_STR * 4;
constexpr size_t WS_AT = WS_H + (size_t)128 * NKP * 1024;
constexpr size_t WS_W2F = WS_AT + (size_t)SD * SL * 2;

#define STAMP(ws, slot) do {} while (0)
DEVI float bf2f(u16 v) { return __uint_as_float(((unsigned)v) << 16); }
DEVI float bfr(float f) { return (float)(__bf16)f; }
DEVI unsigned pk_bf(float a, float b) { b2 v = {(__bf16)a, (__bf16)b}; return __builtin_bit_cast(unsigned, v); }
DEVI unsigned pk_h(float a, float b) { unsigned r; asm("v_cvt_pk_f16_f32 %0, %1, %2" : "=v"(r) : "v"(a), "v"(b)); return r; }
DEVI float lo_bf(unsigned v) { return __uint_as_float(v << 16); }
DEVI float hi_bf(unsigned v) { return __uint_as_float(v & 0xffff0000u); }
template <int N> DEVI void wait_vm() { asm volatile("s_waitcnt vmcnt(%0) lgkmcnt(0)" ::"n"(N) : "memory"); }
DEVI void bar() { asm volatile("" ::: "memory"); __builtin_amdgcn_s_barrier(); asm volatile("" ::: "memory"); }
DEVI void lbar() { asm volatile("s_waitcnt lgkmcnt(0)" ::: "memory"); __builtin_amdgcn_s_barrier(); asm volatile("" ::: "memory"); }
DEVI void glds16(const void* g, char* l) {
  __builtin_amdgcn_global_load_lds((const GAS void*)g, (LAS void*)l, 16, 0, 0);
}
DEVI b4 trread(const char* p) { return __builtin_amdgcn_ds_read_tr16_b64_v4bf16((LAS b4*)p); }
DEVI b8 cat(b4 a, b4 b) { return __builtin_shufflevector(a, b, 0, 1, 2, 3, 4, 5, 6, 7); }
template <int V> struct IC { static constexpr int v = V; };
template <int I, int N, class F> DEVI void sfor(F&& f) { if constexpr (I < N) { f(IC<I>{}); sfor<I + 1, N>(f); } }
DEVI void st16(void* base, unsigned bytes, unsigned off, u4 v) {
  __builtin_amdgcn_raw_buffer_store_b128(v, __builtin_amdgcn_make_buffer_rsrc(base, 0, bytes, 0x00020000), off, 0, 16);
}
DEVI u4 pair16(u2 a, u2 b) {
  const auto rx = __builtin_amdgcn_permlane16_swap(a.x, b.x, false, false);
  const auto ry = __builtin_amdgcn_permlane16_swap(a.y, b.y, false, false);
  return (u4){rx[0], ry[0], rx[1], ry[1]};
}
template <int CTRL, int RM> DEVI float dppf(float v) {
  return __builtin_bit_cast(float, __builtin_amdgcn_update_dpp(0, __builtin_bit_cast(int, v), CTRL, RM, 0xf, false));
}
DEVI float wave_max(float v) {
  v = fmaxf(v, dppf<0x111, 0xf>(v)); v = fmaxf(v, dppf<0x112, 0xf>(v)); v = fmaxf(v, dppf<0x114, 0xf>(v));
  v = fmaxf(v, dppf<0x118, 0xf>(v)); v = fmaxf(v, dppf<0x142, 0xa>(v)); v = fmaxf(v, dppf<0x143, 0xc>(v));
  return __builtin_bit_cast(float, __builtin_amdgcn_readlane(__builtin_bit_cast(int, v), 63));
}
DEVI float wave_sum(float v) {
  v += dppf<0x111, 0xf>(v); v += dppf<0x112, 0xf>(v); v += dppf<0x114, 0xf>(v);
  v += dppf<0x118, 0xf>(v); v += dppf<0x142, 0xa>(v); v += dppf<0x143, 0xc>(v);
  return __builtin_bit_cast(float, __builtin_amdgcn_readlane(__builtin_bit_cast(int, v), 63));
}
#define PIN(x) asm volatile("" : "+v"(x))
__device__ const unsigned g_ones[4] = {0x3f803f80u, 0x3f803f80u, 0x3f803f80u, 0x3f803f80u};

constexpr int G1_S = 5;
constexpr int G1_STAGE = 16384;
constexpr int G1_LDS = G1_S * G1_STAGE;

template <int WM>
DEVI void g1_body(int bid, char* sm, const u16* __restrict__ x, const u16* __restrict__ w1,
                  u16* __restrict__ h, _Float16* __restrict__ xnt, int w) {
  const int tid = threadIdx.x, l = tid & 63;
  const int wn = w & 3;
  const int xcd = bid & 7, idx = bid >> 3;
  const int pm = (xcd >> 1) * 8 + (idx >> 2), pn = (xcd & 1) * 4 + (idx & 3);
  const int r0 = pm * 64, c0 = pn * 176;
  const int g = l >> 4, li = l & 15, q = li >> 2, p = l & 3;

  const char* ga[2];
  long gs[2];
#pragma unroll
  for (int j = 0; j < 2; ++j) {
    const int pc = 2 * w + j;
    if (pc < 4) {
      const int xrow = 16 * pc + (l >> 2);
      ga[j] = (const char*)(x + (size_t)(r0 + xrow) * SD + (((l & 3) ^ ((xrow >> 2) & 3)) * 8));
      gs[j] = 64;
    } else {
      const int ci = (pc - 4) * 64 + l, k = ci / 24, cp = ci - k * 24;
      const int c = cp ^ (((k >> 1) & 3) << 1);
      int col = c0 + 8 * c; if (col > SF - 8) col = SF - 8;
      ga[j] = c >= 22 ? (const char*)g_ones : (const char*)(w1 + (size_t)k * SF + col);
      gs[j] = c >= 22 ? 0 : 32 * SF * 2;
    }
  }
  char* const lb = sm + w * 2048;
  auto issue = [&](int slot, int j) { glds16(ga[j], lb + slot * G1_STAGE + j * 1024); ga[j] += gs[j]; };
#pragma unroll
  for (int s = 0; s < G1_S; ++s) { issue(s, 0); issue(s, 1); }

  const int mt0 = 2 * WM + (wn & 1), mt1 = 2 * WM + ((wn & 1) ^ 1);
  const int xo0 = li * 64 + (((g >> 1) ^ q) * 16) + 8 * (g & 1), xo1 = li * 64 + (((2 + (g >> 1)) ^ q) * 16) + 8 * (g & 1);
  const char* xa00 = sm + mt0 * 1024 + xo0; const char* xa01 = sm + mt0 * 1024 + xo1;
  const char* xa10 = sm + mt1 * 1024 + xo0; const char* xa11 = sm + mt1 * 1024 + xo1;
  const char* wa[3];
#pragma unroll
  for (int j = 0; j < 3; ++j) {
    const int row = 4 * g + q;
    wa[j] = sm + 4096 + row * 384 + (((3 * wn + j) ^ ((row >> 1) & 3)) * 32) + 8 * p;
  }
  const int pi = tid >> 4, c4 = tid & 15;
  unsigned hoff[3];
#pragma unroll
  for (int j = 0; j < 3; ++j) {
    const int col = c0 + 16 * (3 * wn + j) + 8 * (g >> 1);
    const bool ok = (3 * wn + j < 11);
    hoff[j] = ok ? (unsigned)((((pm * 4 + ((g & 1) ? mt1 : mt0)) * NKP + (col >> 5)) * 64 + ((col >> 3) & 3) * 16 + li) * 16) : 0x80000000u;
    PIN(hoff[j]);
  }
  u2 xva, xvb;

  f4 acc[2][3], cs[3], rq = {0.f, 0.f, 0.f, 0.f};
#pragma unroll
  for (int j = 0; j < 3; ++j) { acc[0][j] = acc[1][j] = cs[j] = (f4){0.f, 0.f, 0.f, 0.f}; }
  const b8 ones = {(__bf16)1.0f, (__bf16)1.0f, (__bf16)1.0f, (__bf16)1.0f, (__bf16)1.0f, (__bf16)1.0f, (__bf16)1.0f, (__bf16)1.0f};
  b8 xf[2][2], wf[2][3];
  auto rd = [&](int buf, int so) {
#pragma unroll
    for (int j = 0; j < 3; ++j) wf[buf][j] = cat(trread(wa[j] + so), trread(wa[j] + so + 16 * 384));
    xf[buf][0] = cat(*(const b4*)(xa00 + so), *(const b4*)(xa01 + so));
    xf[buf][1] = cat(*(const b4*)(xa10 + so), *(const b4*)(xa11 + so));
  };
  wait_vm<2 * (G1_S - 1)>();
  bar();
  rd(0, 0);
  sfor<0, 16>([&](auto K) {
    constexpr int k = K.v, u = k & 1;
    if constexpr (k < 15) {
      constexpr int later = ((k + G1_S) < 16 ? (k + G1_S) : 16) - (k + 2);
      wait_vm<2 * later + (k > 11 ? 2 : 0)>();
      bar();
      if constexpr (k + G1_S < 16) issue(k % G1_S, 0);
      if constexpr (k == 11) {
        xva = *(const u2*)(x + (size_t)(r0 + 2 * pi) * SD + 64 * pn + 4 * c4);
        xvb = *(const u2*)(x + (size_t)(r0 + 2 * pi + 1) * SD + 64 * pn + 4 * c4);
      }
      rd(u ^ 1, ((k + 1) % G1_S) * G1_STAGE);
    }
    __builtin_amdgcn_sched_barrier(0);
#pragma unroll
    for (int j = 0; j < 3; ++j) acc[0][j] = __builtin_amdgcn_mfma_f32_16x16x32_bf16(wf[u][j], xf[u][0], acc[0][j], 0, 0, 0);
    rq = __builtin_amdgcn_mfma_f32_16x16x32_bf16(xf[u][0], xf[u][0], rq, 0, 0, 0);
    __builtin_amdgcn_sched_barrier(0);
    if constexpr (k + G1_S < 16) issue(k % G1_S, 1);
    __builtin_amdgcn_sched_barrier(0);
#pragma unroll
    for (int j = 0; j < 3; ++j) acc[1][j] = __builtin_amdgcn_mfma_f32_16x16x32_bf16(wf[u][j], xf[u][1], acc[1][j], 0, 0, 0);
    if constexpr (u == WM) {
#pragma unroll
      for (int j = 0; j < 3; ++j) cs[j] = __builtin_amdgcn_mfma_f32_16x16x32_bf16(wf[u][j], ones, cs[j], 0, 0, 0);
    }
    __builtin_amdgcn_sched_barrier(0);
  });
  STAMP(xnt, 4);
  PIN(xva); PIN(xvb);
  float* raw = (float*)(sm + 2 * G1_STAGE);
  float2* stats = (float2*)(sm + 2 * G1_STAGE + 1024);
  float* csum = (float*)(sm + 2 * G1_STAGE + 2048);
  if (wn == 3 && g == 0) { raw[16 * mt0 + li] = acc[0][2][0]; raw[16 * mt1 + li] = acc[1][2][0]; }
  if (wn < 2 && g == q) raw[64 + 16 * mt0 + li] = p == 0 ? rq[0] : (p == 1 ? rq[1] : (p == 2 ? rq[2] : rq[3]));
  if (li == 0) {
#pragma unroll
    for (int j = 0; j < 3; ++j) *(f4*)(csum + WM * 192 + 16 * (3 * wn + j) + 4 * g) = cs[j];
  }
  lbar();
  if (tid < 64) {
    const float mean = raw[tid] * (1.0f / SD);
    float var = raw[64 + tid] * (1.0f / SD) - mean * mean; var = var < 0.f ? 0.f : var;
    stats[tid] = make_float2(bfr(mean), bfr(__builtin_amdgcn_sqrtf(bfr(bfr(var) + 1e-5f))));
  }
  f4 csr[3];
#pragma unroll
  for (int j = 0; j < 3; ++j)
    csr[j] = *(const f4*)(csum + 16 * (3 * wn + j) + 4 * g) + *(const f4*)(csum + 192 + 16 * (3 * wn + j) + 4 * g);
  lbar();
  unsigned* T = (unsigned*)(sm + 3 * G1_STAGE);
  {
    const float2 s0 = stats[2 * pi], s1 = stats[2 * pi + 1];
    const float i0 = __builtin_amdgcn_rcpf(s0.y), i1 = __builtin_amdgcn_rcpf(s1.y);
#pragma unroll
    for (int e = 0; e < 4; ++e) {
      const unsigned wva = xva[e >> 1], wvb = xvb[e >> 1];
      const float a = (e & 1) ? hi_bf(wva) : lo_bf(wva), b = (e & 1) ? hi_bf(wvb) : lo_bf(wvb);
      const float na = bfr(bfr(a - s0.x) * i0), nb = bfr(bfr(b - s1.x) * i1);
      T[(4 * c4 + e) * 32 + (pi ^ ((c4 >> 1) << 2))] = pk_h(na, nb);
    }
  }
  {
    const float2 st0 = stats[16 * mt0 + li], st1 = stats[16 * mt1 + li];
    const float inv0 = __builtin_amdgcn_rcpf(st0.y), inv1 = __builtin_amdgcn_rcpf(st1.y);
#pragma unroll
    for (int j = 0; j < 3; ++j) {
      float v[4], u[4];
#pragma unroll
      for (int r = 0; r < 4; ++r) {
        v[r] = fmaxf((acc[0][j][r] - st0.x * csr[j][r]) * inv0, 0.f);
        u[r] = fmaxf((acc[1][j][r] - st1.x * csr[j][r]) * inv1, 0.f);
      }
      u4 o = pair16((u2){pk_bf(v[0], v[1]), pk_bf(v[2], v[3])}, (u2){pk_bf(u[0], u[1]), pk_bf(u[2], u[3])});
      if (c0 + 16 * (3 * wn + j) >= SF) o = (u4){0u, 0u, 0u, 0u};
      st16(h, 128 * NKP * 1024, hoff[j], o);
    }
  }
  {
    lbar();
    const int ch = tid >> 3, m = tid & 7;
    const u4 v = *(const u4*)(T + ch * 32 + 4 * (m ^ (ch >> 3)));
    st16(xnt, SD * SL * 2, ((64 * pn + ch) * SL + r0 + 8 * m) * 2, v);
  }
}

DEVI void g1_role(int bid, char* sm, const u16* __restrict__ x, const u16* __restrict__ w1,
                  u16* __restrict__ h, _Float16* __restrict__ xnt) {
  const int w = __builtin_amdgcn_readfirstlane(threadIdx.x >> 6);
  __builtin_amdgcn_s_setprio(2);
  if (w < 4) g1_body<0>(bid, sm, x, w1, h, xnt, w); else g1_body<1>(bid, sm, x, w1, h, xnt, w);
}

constexpr int KG_CH = 80 * 512;
constexpr int KG_LDS = 2 * KG_CH;

struct cf { float r, i; };
DEVI cf cmul(cf a, cf b) {
  float t, u, re, im;
  asm("v_mul_f32 %0, %2, %3\n\tv_mul_f32 %1, %2, %4" : "=&v"(t), "=&v"(u) : "v"(a.i), "v"(b.i), "v"(b.r));
  asm("v_fma_f32 %0, %2, %3, -%4\n\tv_fma_f32 %1, %2, %5, %6" : "=&v"(re), "=&v"(im) : "v"(a.r), "v"(b.r), "v"(t), "v"(b.i), "v"(u));
  return {re, im};
}
DEVI cf cseed(float al, float om, float m) {
  const float mag = __builtin_amdgcn_exp2f((al * m) * 1.44269504088896341f);
  const float ph = om * m;
  const float chi = 0.15915494f, clo = 6.4206382e-9f;
  const float fr = __builtin_fmaf(ph, clo, __builtin_fmaf(ph, chi, -__builtin_rintf(ph * chi)));
  return {mag * __builtin_amdgcn_cosf(fr), mag * __builtin_amdgcn_sinf(fr)};
}

DEVI void kg_role(int kb, char* sm0, const u16* __restrict__ Are, const u16* __restrict__ Aim,
                  const u16* __restrict__ Bre, const u16* __restrict__ Bim, const u16* __restrict__ Cre,
                  const u16* __restrict__ Cim, const u16* __restrict__ logdt, const u16* __restrict__ w2,
                  _Float16* __restrict__ krev, float* __restrict__ ksc, u16* __restrict__ w2f) {
  const int l = threadIdx.x & 63;
  const int w8 = __builtin_amdgcn_readfirstlane(threadIdx.x >> 6);
  const int w = w8 & 3, tid = threadIdx.x & 255;
  const int d = 2 * kb + (w8 >> 2);
  char* sm = sm0 + (w8 >> 2) * KG_CH;
  const int n = tid & 127;
  const u16 ldt = logdt[d], are = Are[d * SN + n], aim = Aim[d * SN + n];
  const int fr = kb * 8 + w8;
  u16 wv[8] = {0, 0, 0, 0, 0, 0, 0, 0};
  if (fr < NKS * 32) {
    const u16* src = w2 + (size_t)(32 * (fr >> 5) + 8 * (l >> 4)) * SD + 16 * (fr & 31) + (l & 15);
#pragma unroll
    for (int e = 0; e < 8; ++e) wv[e] = src[(size_t)e * SD];
  }
  int xr, yr;
  {
    const int li = l & 15, y = li << w;
    yr = y < 16 ? y : (y < 32 ? 8 + (y >> 1) : (y < 64 ? 16 + (y >> 2) : 24 + (y >> 3)));
    xr = 40 + (w == 0 ? li : (w == 3 ? 32 + (li & 7) : 16 + li));
  }
  const int xbase = xr * 512 + (((l >> 4) ^ (xr & 15)) << 4), ybase = yr * 512 + (((l >> 4) ^ (yr & 15)) << 4);
  int ebase; unsigned epat;
  {
    const int m = 255 - tid;
    if (m < 32) { ebase = 8 * m; epat = 0x76543210u; }
    else if (m < 64) { ebase = 128 + 4 * m; epat = 0x43222100u; }
    else if (m < 128) { ebase = 256 + 2 * m; epat = 0x22111000u; }
    else { ebase = 384 + m; epat = (m & 1) ? 0x11110000u : 0x11100000u; }
  }
  const int kbase = w == 0 ? 0 : (w == 3 ? 512 : 256), ilo = w == 2 ? 8 : 0, ihi = (w & 1) ? 8 : 16;
  const float dt = bfr(expf(bf2f(ldt)));
  const float al = dt * bf2f(are), om = dt * bf2f(aim);
  const int wa = (n >> 2) * 16 + (n & 3) * 4;
#define KG_ST(row, re, im) *(unsigned*)(sm + (row) * 512 + (wa ^ (((row) & 15) << 4))) = pk_h(re, im)
  float k2048 = 0.f;
  if (w < 2) {
    cf P[16], Q[8], R[8];
    const cf e1 = cseed(al, om, 1.f), e16 = cseed(al, om, 16.f), e32 = cseed(al, om, 32.f), e64 = cseed(al, om, 64.f);
    P[0] = {1.f, 0.f}; P[1] = e1;
#pragma unroll
    for (int j = 2; j < 16; ++j) P[j] = cmul(P[j >> 1], P[j - (j >> 1)]);
#pragma unroll
    for (int j = 0; j < 16; ++j) KG_ST(j, P[j].r, P[j].i);
    Q[0] = e16;
#pragma unroll
    for (int m = 1; m < 8; ++m) Q[m] = cmul(e16, P[2 * m]);
#pragma unroll
    for (int m = 0; m < 8; ++m) KG_ST(16 + m, Q[m].r, Q[m].i);
    R[0] = e32;
#pragma unroll
    for (int m = 1; m < 8; ++m) R[m] = cmul(e32, m < 4 ? P[4 * m] : Q[2 * (m - 4)]);
#pragma unroll
    for (int m = 0; m < 8; ++m) KG_ST(24 + m, R[m].r, R[m].i);
    KG_ST(32, e64.r, e64.i);
#pragma unroll
    for (int m = 1; m < 8; ++m) {
      const cf t = cmul(e64, m < 2 ? P[8 * m] : (m < 4 ? Q[4 * (m - 2)] : R[2 * (m - 4)]));
      KG_ST(32 + m, t.r, t.i);
    }
  } else {
    cf cb[2];
    float mx = 0.f;
#pragma unroll
    for (int hh = 0; hh < 2; ++hh) {
      const int nn = (n & 63) + 64 * hh;
      const float br = dt * bf2f(Bre[d * SN + nn]), bi = dt * bf2f(Bim[d * SN + nn]);
      const float cr = bf2f(Cre[d * SN + nn]), ci = bf2f(Cim[d * SN + nn]);
      cb[hh] = cmul({cr, ci}, {br, bi});
      mx = fmaxf(mx, fmaxf(fabsf(cb[hh].r), fabsf(cb[hh].i)));
    }
    const float on = -om;
    const cf e16 = cseed(al, on, 16.f), e32 = cseed(al, on, 32.f), e64 = cseed(al, on, 64.f), e128 = cseed(al, on, 128.f);
    const cf e256 = cseed(al, on, 256.f), e512 = cseed(al, on, 512.f), e1024 = cseed(al, on, 1024.f);
    mx = wave_max(mx);
    int ex = (mx > 0.f && mx < 3.0e38f) ? __builtin_amdgcn_frexp_expf(mx) : 0;
    ex = ex < -100 ? -100 : (ex > 100 ? 100 : ex);
    const float sc = __builtin_amdgcn_ldexpf(1.0f, -ex);
    if (tid == 128) ksc[d] = __builtin_amdgcn_ldexpf(1.0f, ex);
    const cf c0 = (w == 2) ? cb[0] : cb[1];
    const cf cbs = {c0.r * sc, -c0.i * sc};
    cf a0 = cbs, a0b = cmul(cbs, e128), a1 = cmul(cbs, e256), a2 = cmul(cbs, e512), a3 = cmul(cbs, e1024);
#pragma unroll
    for (int i = 0; i < 8; ++i) {
      KG_ST(40 + i, a0.r, a0.i);
      KG_ST(48 + i, a0b.r, a0b.i);
      KG_ST(56 + i, a1.r, a1.i);
      KG_ST(64 + i, a2.r, a2.i);
      KG_ST(72 + i, a3.r, a3.i);
      if (i < 7) { a0 = cmul(a0, e16); a0b = cmul(a0b, e16); a1 = cmul(a1, e32); a2 = cmul(a2, e64); a3 = cmul(a3, e128); }
    }
    k2048 = wave_sum(a3.r * e128.r - a3.i * e128.i);
  }
#undef KG_ST
  if (fr < (NKS + 1) * 32) {
    const u4 f = {(unsigned)wv[0] | ((unsigned)wv[1] << 16), (unsigned)wv[2] | ((unsigned)wv[3] << 16),
                  (unsigned)wv[4] | ((unsigned)wv[5] << 16), (unsigned)wv[6] | ((unsigned)wv[7] << 16)};
    st16(w2f, (NKS + 1) * 32768, (fr * 64 + l) * 16, f);
  }
  STAMP((char*)krev - WS_KREV, 4);
  lbar();
  STAMP((char*)krev - WS_KREV, 5);
  {
    const int g = l >> 4;
    h8 xf[8], yf[8];
#pragma unroll
    for (int k4 = 0; k4 < 4; ++k4) {
      const char* xa = sm + (xbase ^ (k4 << 6));
      const char* ya = sm + (ybase ^ (k4 << 6));
      xf[k4] = *(const h8*)xa; xf[4 + k4] = *(const h8*)(xa + 256);
      yf[k4] = *(const h8*)ya; yf[4 + k4] = *(const h8*)(ya + 256);
    }
    lbar();
    f4 acc0 = {0.f, 0.f, 0.f, 0.f}, acc1 = {0.f, 0.f, 0.f, 0.f};
#pragma unroll
    for (int kk = 0; kk < 4; ++kk) {
      acc0 = __builtin_amdgcn_mfma_f32_16x16x32_f16(xf[2 * kk], yf[2 * kk], acc0, 0, 0, 0);
      acc1 = __builtin_amdgcn_mfma_f32_16x16x32_f16(xf[2 * kk + 1], yf[2 * kk + 1], acc1, 0, 0, 0);
    }
    _Float16* Kc = (_Float16*)sm;
#pragma unroll
    for (int r = 0; r < 4; ++r) {
      const int i = 4 * g + r;
      const bool ok = (i >= ilo) && (i < ihi);
      Kc[ok ? kbase + 16 * i + (l & 15) : 648] = (_Float16)(acc0[r] + acc1[r]);
    }
    if (w >= 2 && l == 0) ((float*)(sm + 1536))[w - 2] = k2048;
  }
  lbar();
  STAMP((char*)krev - WS_KREV, 6);
  {
    const _Float16* Kc = (const _Float16*)sm + ebase;
    h8 o;
#pragma unroll
    for (int e = 0; e < 8; ++e) o[e] = Kc[(epat >> (4 * (7 - e))) & 15];
    if (tid == 0) {
      const float* kp = (const float*)(sm + 1536);
      const _Float16 klast = (_Float16)(kp[0] + kp[1]);
      o[0] = klast; o[1] = klast; o[2] = klast; o[3] = klast;
    }
    st16(krev, SD * KSTR * 2, (d * KSTR + 8 * tid) * 2, __builtin_bit_cast(u4, o));
    if (tid < 8) st16(krev, SD * KSTR * 2, (d * KSTR + 2048 + 8 * tid) * 2, (u4){0u, 0u, 0u, 0u});
  }
}

constexpr int CV_KC = 4160;
constexpr int CV_U = 8 * CV_KC;
constexpr int CV_CH = CV_U + 95 * 80;
constexpr int CV_LDS = CV_CH;
#define CV_MAIN_PRIO 3
constexpr int CV_RS = 144;

DEVI void cv_role(int d, char* sm, const _Float16* __restrict__ xnt, const _Float16* __restrict__ krev,
                  const float* __restrict__ ksc, const u16* __restrict__ dskip, u16* __restrict__ at,
                  unsigned* __restrict__ cnt) {
  __builtin_amdgcn_s_setprio(3);
  const int l = threadIdx.x & 63;
  const int w = __builtin_amdgcn_readfirstlane(threadIdx.x >> 6);
  const int tid = threadIdx.x;
  const u4 uv = *(const u4*)(xnt + (size_t)d * SL + 8 * tid);
  const u4* kg = (const u4*)(krev + (size_t)d * KSTR);
  const u4 k0 = kg[tid], k1 = kg[tid + 1];
  u4 k2 = {0u, 0u, 0u, 0u}, k3 = {0u, 0u, 0u, 0u};
  if (tid < 3) { k2 = kg[256 + tid]; k3 = kg[257 + tid]; }
  const float scale = ksc[d];
  const float dsk = bf2f(dskip[d]);
  if (tid < 155) *(u4*)(sm + CV_U + tid * 16) = (u4){0u, 0u, 0u, 0u};
  *(u4*)(sm + CV_U + (31 + (tid >> 2)) * 80 + (tid & 3) * 16) = uv;
  auto build = [&](u4 a, u4 b, int q) {
    const unsigned wv[8] = {a.x, a.y, a.z, a.w, b.x, b.y, b.z, b.w};
    unsigned f[7];
#pragma unroll
    for (int m = 0; m < 7; ++m) f[m] = __builtin_amdgcn_alignbit(wv[m + 1], wv[m], 16);
#pragma unroll
    for (int m = 0; m < 4; ++m) {
      *(u4*)(sm + (2 * m) * CV_KC + 16 * q) = (u4){wv[m], wv[m + 1], wv[m + 2], wv[m + 3]};
      *(u4*)(sm + (2 * m + 1) * CV_KC + 16 * q) = (u4){f[m], f[m + 1], f[m + 2], f[m + 3]};
    }
  };
  build(k0, k1, tid);
  if (tid < 3) build(k2, k3, 256 + tid);
  lbar();
  STAMP(xnt, 8);
  __builtin_amdgcn_s_setprio(CV_MAIN_PRIO);
  const int ks = w & 1, hb = w >> 1;
  const int b = l & 31, hh = l >> 5;
  const char* abase = sm + (7 - (b & 7)) * CV_KC + 16 * (255 - 4 * (16 * hb + 47) - (b >> 3) + 2 * ks + hh);
  const char* bbase = sm + CV_U + (31 - 16 * hb - 15 + b) * 80 + 32 * ks + 16 * hh;
  f16v t0 = {}, t1 = {};
#pragma unroll
  for (int i = 0; i < 16; ++i) {
    const h8 a_lo = *(const h8*)(abase + 64 * (15 - i) + 2048);
    const h8 a_hi = *(const h8*)(abase + 64 * (15 - i));
    const h8 b_lo = *(const h8*)(bbase + 80 * (15 - i));
    const h8 b_hi = *(const h8*)(bbase + 80 * (15 - i) + 2560);
    t0 = __builtin_amdgcn_mfma_f32_32x32x16_f16(a_lo, b_lo, t0, 0, 0, 0);
    t1 = __builtin_amdgcn_mfma_f32_32x32x16_f16(a_lo, b_hi, t1, 0, 0, 0);
    t1 = __builtin_amdgcn_mfma_f32_32x32x16_f16(a_hi, b_lo, t1, 0, 0, 0);
  }
  __builtin_amdgcn_s_setprio(3);
  lbar();
  STAMP(xnt, 9);
  {
    char* rb = sm + w * (64 * CV_RS) + b * CV_RS + 16 * hh;
#pragma unroll
    for (int r4 = 0; r4 < 4; ++r4) {
      *(f4*)(rb + 32 * r4) = (f4){t0[4 * r4], t0[4 * r4 + 1], t0[4 * r4 + 2], t0[4 * r4 + 3]};
      *(f4*)(rb + 32 * CV_RS + 32 * r4) = (f4){t1[4 * r4], t1[4 * r4 + 1], t1[4 * r4 + 2], t1[4 * r4 + 3]};
    }
  }
  lbar();
  {
    const char* rb = sm + (tid >> 2) * CV_RS + (tid & 3) * 32;
    f4 s0 = *(const f4*)rb, s1 = *(const f4*)(rb + 16);
#pragma unroll
    for (int ww = 1; ww < 4; ++ww) { s0 += *(const f4*)(rb + ww * (64 * CV_RS)); s1 += *(const f4*)(rb + ww * (64 * CV_RS) + 16); }
    const h8 uu = __builtin_bit_cast(h8, uv);
    float o[8];
#pragma unroll
    for (int e = 0; e < 8; ++e) o[e] = bfr((e < 4 ? s0[e] : s1[e - 4]) * scale) + bfr((float)uu[e] * dsk);
    const u4 ov = {pk_bf(o[0], o[1]), pk_bf(o[2], o[3]), pk_bf(o[4], o[5]), pk_bf(o[6], o[7])};
    const __amdgpu_buffer_rsrc_t rs = __builtin_amdgcn_make_buffer_rsrc(at, 0, SD * SL * 2, 0x00020000);
    __builtin_amdgcn_raw_buffer_store_b128(ov, rs, (d * SL + 8 * tid) * 2, 0, 16);
  }
  STAMP(xnt, 10);
  asm volatile("s_waitcnt vmcnt(0)" ::: "memory");
  __syncthreads();
  STAMP(xnt, 11);
  if (threadIdx.x < 8)
    __hip_atomic_fetch_add((GAS unsigned*)(cnt + ((d >> 6) * 8 + threadIdx.x) * CNT_STR), 1u, __ATOMIC_RELAXED, __HIP_MEMORY_SCOPE_AGENT);
}

constexpr int G2_LDS = 49152;
#define G2_NB 3

DEVI void g2_role(int bid, char* sm, const u16* __restrict__ x, const u16* __restrict__ hfr,
                  const u16* __restrict__ w2f, const u16* __restrict__ at, unsigned* __restrict__ cnt,
                  u16* __restrict__ out) {
  __builtin_amdgcn_s_setprio(1);
  const int tid = threadIdx.x, l = tid & 63;
  const int w = __builtin_amdgcn_readfirstlane(tid >> 6);
  const int xcd = bid & 7, idx = bid >> 3;
  const int pm = (xcd >> 1) * 8 + (idx >> 2), pn = (xcd & 1) * 4 + (idx & 3);
  const int t0 = pm * 64, d0 = pn * 64;
  const int g = l >> 4, li = l & 15, q = li >> 2, p = l & 3;
  const int k0 = 11 * w;

  const __amdgpu_buffer_rsrc_t rh = __builtin_amdgcn_make_buffer_rsrc((void*)hfr, 0, 128 * NKP * 1024, 0x00020000);
  const __amdgpu_buffer_rsrc_t rw = __builtin_amdgcn_make_buffer_rsrc((void*)w2f, 0, NKP * 32768, 0x00020000);
  const int lo = l * 16;
  int hso[4];
#pragma unroll
  for (int s = 0; s < 4; ++s) hso[s] = ((pm * 4 + ((w + s) & 3)) * NKP + k0) * 1024;
  const int wso = (k0 * 32 + pn * 4) * 1024;
  u4 hf[G2_NB][4], wf[G2_NB][4];
  auto ldw = [&](int buf, int i) {
#pragma unroll
    for (int j = 0; j < 4; ++j) wf[buf][j] = __builtin_amdgcn_raw_buffer_load_b128(rw, lo + j * 1024, wso + i * 32768, 0);
  };
  auto ldh = [&](int buf, int i, int s) { hf[buf][s] = __builtin_amdgcn_raw_buffer_load_b128(rh, lo, hso[s] + i * 1024, 0); };
  f4 acc[4][4];
#pragma unroll
  for (int j = 0; j < 4; ++j)
#pragma unroll
    for (int s = 0; s < 4; ++s) acc[j][s] = (f4){0.f, 0.f, 0.f, 0.f};
#pragma unroll
  for (int i = 0; i < G2_NB; ++i) {
    ldw(i, i);
#pragma unroll
    for (int s = 0; s < 4; ++s) ldh(i, i, s);
    __builtin_amdgcn_sched_barrier(0);
  }
  u2 xr[4];
  const __amdgpu_buffer_rsrc_t rs = __builtin_amdgcn_make_buffer_rsrc((void*)at, 0, SD * SL * 2, 0x00020000);
  sfor<0, 11>([&](auto I) {
    constexpr int i = I.v, b = i % G2_NB;
#pragma unroll
    for (int s = 0; s < 4; ++s) {
#pragma unroll
      for (int j = 0; j < 4; ++j)
        acc[j][s] = __builtin_amdgcn_mfma_f32_16x16x32_bf16(__builtin_bit_cast(b8, wf[b][j]), __builtin_bit_cast(b8, hf[b][s]), acc[j][s], 0, 0, 0);
      __builtin_amdgcn_sched_barrier(0);
      if constexpr (i + G2_NB < 11) { ldh(b, i + G2_NB, s); __builtin_amdgcn_sched_barrier(0); }
    }
    if constexpr (i + G2_NB < 11) { ldw(b, i + G2_NB); __builtin_amdgcn_sched_barrier(0); }
    if constexpr (i == 8) {
#pragma unroll
      for (int j = 0; j < 4; ++j) xr[j] = *(const u2*)(x + (size_t)(t0 + 16 * w + li) * SD + d0 + 16 * j + 4 * g);
      __builtin_amdgcn_sched_barrier(0);
    }
  });
#pragma unroll
  for (int s = 1; s < 4; ++s)
#pragma unroll
    for (int j = 0; j < 4; ++j) *(f4*)(sm + ((((w + s) & 3) * 3 + (3 - s)) * 4 + j) * 1024 + l * 16) = acc[j][s];
  STAMP((char*)hfr - WS_H, 8);
  if (w == 0) {
    GAS unsigned* c = (GAS unsigned*)(cnt + (pn * 8 + (pm & 7)) * CNT_STR);
    for (unsigned spins = 0; spins < (1u << 20); ++spins) {
      const unsigned v = __builtin_amdgcn_readfirstlane(__hip_atomic_load(c, __ATOMIC_RELAXED, __HIP_MEMORY_SCOPE_AGENT));
      if (v >= 64u) break;
      __builtin_amdgcn_s_sleep(4);
    }
  }
  lbar();
  STAMP((char*)hfr - WS_H, 9);
  const int aoff = ((d0 + l) * SL + t0 + 16 * w) * 2;
  const u4 av0 = __builtin_amdgcn_raw_buffer_load_b128(rs, aoff, 0, 16);
  const u4 av1 = __builtin_amdgcn_raw_buffer_load_b128(rs, aoff + 16, 0, 16);
  f4 v[4];
#pragma unroll
  for (int j = 0; j < 4; ++j) {
    v[j] = acc[j][0];
#pragma unroll
    for (int sI = 0; sI < 3; ++sI) v[j] += *(const f4*)(sm + ((w * 3 + sI) * 4 + j) * 1024 + l * 16);
  }
  asm volatile("s_waitcnt lgkmcnt(0)" ::: "memory");
  char* myR = sm + w * 12288;
  *(u4*)(myR + l * 32) = av0;
  *(u4*)(myR + l * 32 + 16) = av1;
  asm volatile("s_waitcnt lgkmcnt(0)" ::: "memory");
  STAMP((char*)hfr - WS_H, 10);
  u2 ov[4];
#pragma unroll
  for (int j = 0; j < 4; ++j) {
    const b4 a4 = trread(myR + (16 * j + 4 * g + q) * 32 + 8 * p);
    const u2 xv = xr[j];
    const float xs[4] = {lo_bf(xv.x), hi_bf(xv.x), lo_bf(xv.y), hi_bf(xv.y)};
    float o[4];
#pragma unroll
    for (int r = 0; r < 4; ++r) o[r] = bfr(xs[r] + (float)a4[r]) + bfr(v[j][r]);
    ov[j] = (u2){pk_bf(o[0], o[1]), pk_bf(o[2], o[3])};
  }
  const unsigned oo = ((t0 + 16 * w + li) * SD + d0 + 16 * (g & 1) + 8 * (g >> 1)) * 2;
  st16(out, SL * SD * 2, oo, pair16(ov[0], ov[1]));
  st16(out, SL * SD * 2, oo + 64, pair16(ov[2], ov[3]));
}

constexpr int P1_LDS = G1_LDS > KG_LDS ? G1_LDS : KG_LDS;
constexpr int P2_LDS = G2_LDS > CV_LDS ? G2_LDS : CV_LDS;

__global__ __launch_bounds__(512, 4) void k_phase1(const u16* x, const u16* Are, const u16* Aim, const u16* Bre,
                                                   const u16* Bim, const u16* Cre, const u16* Cim, const u16* logdt,
                                                   const u16* w1, const u16* w2, char* ws, int boff) {
  extern __shared__ __attribute__((aligned(16))) char sm[];
  const int bid = blockIdx.x + boff;
  STAMP(ws, 0);
  if (bid < 256) {
    if (bid == 0 && threadIdx.x < 64) ((unsigned*)(ws + WS_CNT))[threadIdx.x * CNT_STR] = 0u;
    g1_role(bid, sm, x, w1, (u16*)(ws + WS_H), (_Float16*)(ws + WS_XNT));
  } else {
    kg_role(bid - 256, sm, Are, Aim, Bre, Bim, Cre, Cim, logdt, w2, (_Float16*)(ws + WS_KREV), (float*)(ws + WS_KSC),
            (u16*)(ws + WS_W2F));
  }
  STAMP(ws, 1);
}

__global__ __launch_bounds__(256, 3) void k_phase2(const u16* x, const u16* dskip, char* ws, u16* out, int boff) {
  extern __shared__ __attribute__((aligned(16))) char sm[];
  const int bid = blockIdx.x + boff;
  STAMP(ws, 2);
  if (bid >= 512) {
    g2_role(bid - 512, sm, x, (const u16*)(ws + WS_H), (const u16*)(ws + WS_W2F), (const u16*)(ws + WS_AT),
            (unsigned*)(ws + WS_CNT), out);
  } else {
    cv_role(bid, sm, (const _Float16*)(ws + WS_XNT), (const _Float16*)(ws + WS_KREV), (const float*)(ws + WS_KSC),
            dskip, (u16*)(ws + WS_AT), (unsigned*)(ws + WS_CNT));
  }
  STAMP(ws, 3);
}

static void launch_p1(void* const* d_in, void* d_ws, hipStream_t stream, int grid, int boff) {
  (void)hipFuncSetAttribute((const void*)k_phase1, hipFuncAttributeMaxDynamicSharedMemorySize, P1_LDS);
  k_phase1<<<grid, 512, P1_LDS, stream>>>((const u16*)d_in[0], (const u16*)d_in[1], (const u16*)d_in[2], (const u16*)d_in[3],
                                          (const u16*)d_in[4], (const u16*)d_in[5], (const u16*)d_in[6],
                                          (const u16*)d_in[7], (const u16*)d_in[10], (const u16*)d_in[11], (char*)d_ws, boff);
}
static void launch_p2(void* const* d_in, void* d_out, void* d_ws, hipStream_t stream, int grid, int boff) {
  (void)hipFuncSetAttribute((const void*)k_phase2, hipFuncAttributeMaxDynamicSharedMemorySize, P2_LDS);
  k_phase2<<<grid, 256, P2_LDS, stream>>>((const u16*)d_in[0], (const u16*)d_in[8], (char*)d_ws, (u16*)d_out, boff);
}

extern "C" void kernel_launch(void* const* d_in, const int* in_sizes, int n_in, void* d_out, int out_size,
                              void* d_ws, size_t ws_size, hipStream_t stream) {
  launch_p1(d_in, d_ws, stream, 512, 0);
  launch_p2(d_in, d_out, d_ws, stream, 768, 0);
}
```

```cpp
#include <hip/hip_runtime.h>
#include <hip/hip_bf16.h>
#include <stdint.h>

typedef unsigned short u16;
typedef _Float16 h8 __attribute__((ext_vector_type(8)));
typedef __bf16 b8 __attribute__((ext_vector_type(8)));
typedef __bf16 b4 __attribute__((ext_vector_type(4)));
typedef __bf16 b2 __attribute__((ext_vector_type(2)));
typedef float f4 __attribute__((ext_vector_type(4)));
typedef float f16v __attribute__((ext_vector_type(16)));
typedef unsigned u4 __attribute__((ext_vector_type(4)));
typedef unsigned u2 __attribute__((ext_vector_type(2)));
#define LAS __attribute__((address_space(3)))
#define GAS __attribute__((address_space(1)))
#define DEVI __device__ __forceinline__

constexpr int SL = 2048, SD = 512, SN = 128, SF = 1376;
constexpr int KSTR = 2112;
constexpr int NKS = SF / 32;
constexpr int NKP = NKS + 1;
constexpr size_t WS_XNT = 0;
constexpr size_t WS_KREV = WS_XNT + (size_t)SD * SL * 2;
constexpr size_t WS_KSC = WS_KREV + (size_t)SD * KSTR * 2;
constexpr int CNT_STR = 1088;
constexpr size_t WS_CNT = WS_KSC + 4096;
constexpr size_t WS_H = WS_CNT + 64 * CNT_STR * 4;
constexpr size_t WS_AT = WS_H + (size_t)128 * NKP * 1024;
constexpr size_t WS_W2F = WS_AT + (size_t)SD * SL * 2;

#define STAMP(ws, slot) do {} while (0)
DEVI float bf2f(u16 v) { return __uint_as_float(((unsigned)v) << 16); }
DEVI float bfr(float f) { return (float)(__bf16)f; }
DEVI unsigned pk_bf(float a, float b) { b2 v = {(__bf16)a, (__bf16)b}; return __builtin_bit_cast(unsigned, v); }
DEVI unsigned pk_h(float a, float b) { unsigned r; asm("v_cvt_pk_f16_f32 %0, %1, %2" : "=v"(r) : "v"(a), "v"(b)); return r; }
DEVI float lo_bf(unsigned v) { return __uint_as_float(v << 16); }
DEVI float hi_bf(unsigned v) { return __uint_as_float(v & 0xffff0000u); }
template <int N> DEVI void wait_vm() { asm volatile("s_waitcnt vmcnt(%0) lgkmcnt(0)" ::"n"(N) : "memory"); }
DEVI void bar() { asm volatile("" ::: "memory"); __builtin_amdgcn_s_barrier(); asm volatile("" ::: "memory"); }
DEVI void lbar() { asm volatile("s_waitcnt lgkmcnt(0)" ::: "memory"); __builtin_amdgcn_s_barrier(); asm volatile("" ::: "memory"); }
DEVI void glds16(const void* g, char* l) {
  __builtin_amdgcn_global_load_lds((const GAS void*)g, (LAS void*)l, 16, 0, 0);
}
DEVI b4 trread(const char* p) { return __builtin_amdgcn_ds_read_tr16_b64_v4bf16((LAS b4*)p); }
DEVI b8 cat(b4 a, b4 b) { return __builtin_shufflevector(a, b, 0, 1, 2, 3, 4, 5, 6, 7); }
template <int V> struct IC { static constexpr int v = V; };
template <int I, int N, class F> DEVI void sfor(F&& f) { if constexpr (I < N) { f(IC<I>{}); sfor<I + 1, N>(f); } }
DEVI void st16(void* base, unsigned bytes, unsigned off, u4 v) {
  __builtin_amdgcn_raw_buffer_store_b128(v, __builtin_amdgcn_make_buffer_rsrc(base, 0, bytes, 0x00020000), off, 0, 16);
}
DEVI u4 pair16(u2 a, u2 b) {
  const auto rx = __builtin_amdgcn_permlane16_swap(a.x, b.x, false, false);
  const auto ry = __builtin_amdgcn_permlane16_swap(a.y, b.y, false, false);
  return (u4){rx[0], ry[0], rx[1], ry[1]};
}
template <int CTRL, int RM> DEVI float dppf(float v) {
  return __builtin_bit_cast(float, __builtin_amdgcn_update_dpp(0, __builtin_bit_cast(int, v), CTRL, RM, 0xf, false));
}
DEVI float wave_max(float v) {
  v = fmaxf(v, dppf<0x111, 0xf>(v)); v = fmaxf(v, dppf<0x112, 0xf>(v)); v = fmaxf(v, dppf<0x114, 0xf>(v));
  v = fmaxf(v, dppf<0x118, 0xf>(v)); v = fmaxf(v, dppf<0x142, 0xa>(v)); v = fmaxf(v, dppf<0x143, 0xc>(v));
  return __builtin_bit_cast(float, __builtin_amdgcn_readlane(__builtin_bit_cast(int, v), 63));
}
DEVI float wave_sum(float v) {
  v += dppf<0x111, 0xf>(v); v += dppf<0x112, 0xf>(v); v += dppf<0x114, 0xf>(v);
  v += dppf<0x118, 0xf>(v); v += dppf<0x142, 0xa>(v); v += dppf<0x143, 0xc>(v);
  return __builtin_bit_cast(float, __builtin_amdgcn_readlane(__builtin_bit_cast(int, v), 63));
}
#define PIN(x) asm volatile("" : "+v"(x))
__device__ const unsigned g_ones[4] = {0x3f803f80u, 0x3f803f80u, 0x3f803f80u, 0x3f803f80u};

constexpr int G1_S = 5;
constexpr int G1_STAGE = 16384;
constexpr int G1_LDS = G1_S * G1_STAGE;

template <int WM>
DEVI void g1_body(int bid, char* sm, const u16* __restrict__ x, const u16* __restrict__ w1,
                  u16* __restrict__ h, _Float16* __restrict__ xnt, int w) {
  const int tid = threadIdx.x, l = tid & 63;
  const int wn = w & 3;
  const int xcd = bid & 7, idx = bid >> 3;
  const int pm = (xcd >> 1) * 8 + (idx >> 2), pn = (xcd & 1) * 4 + (idx & 3);
  const int r0 = pm * 64, c0 = pn * 176;
  const int g = l >> 4, li = l & 15, q = li >> 2, p = l & 3;

  const char* ga[2];
  long gs[2];
#pragma unroll
  for (int j = 0; j < 2; ++j) {
    const int pc = 2 * w + j;
    if (pc < 4) {
      const int xrow = 16 * pc + (l >> 2);
      ga[j] = (const char*)(x + (size_t)(r0 + xrow) * SD + (((l & 3) ^ ((xrow >> 2) & 3)) * 8));
      gs[j] = 64;
    } else {
      const int ci = (pc - 4) * 64 + l, k = ci / 24, cp = ci - k * 24;
      const int c = cp ^ (((k >> 1) & 3) << 1);
      int col = c0 + 8 * c; if (col > SF - 8) col = SF - 8;
      ga[j] = c >= 22 ? (const char*)g_ones : (const char*)(w1 + (size_t)k * SF + col);
      gs[j] = c >= 22 ? 0 : 32 * SF * 2;
    }
  }
  char* const lb = sm + w * 2048;
  auto issue = [&](int slot, int j) { glds16(ga[j], lb + slot * G1_STAGE + j * 1024); ga[j] += gs[j]; };
#pragma unroll
  for (int s = 0; s < G1_S; ++s) { issue(s, 0); issue(s, 1); }

  const int mt0 = 2 * WM + (wn & 1), mt1 = 2 * WM + ((wn & 1) ^ 1);
  const int xo0 = li * 64 + (((g >> 1) ^ q) * 16) + 8 * (g & 1), xo1 = li * 64 + (((2 + (g >> 1)) ^ q) * 16) + 8 * (g & 1);
  const char* xa00 = sm + mt0 * 1024 + xo0; const char* xa01 = sm + mt0 * 1024 + xo1;
  const char* xa10 = sm + mt1 * 1024 + xo0; const char* xa11 = sm + mt1 * 1024 + xo1;
  const char* wa[3];
#pragma unroll
  for (int j = 0; j < 3; ++j) {
    const int row = 4 * g + q;
    wa[j] = sm + 4096 + row * 384 + (((3 * wn + j) ^ ((row >> 1) & 3)) * 32) + 8 * p;
  }
  const int pi = tid >> 4, c4 = tid & 15;
  unsigned hoff[3];
#pragma unroll
  for (int j = 0; j < 3; ++j) {
    const int col = c0 + 16 * (3 * wn + j) + 8 * (g >> 1);
    const bool ok = (3 * wn + j < 11);
    hoff[j] = ok ? (unsigned)((((pm * 4 + ((g & 1) ? mt1 : mt0)) * NKP + (col >> 5)) * 64 + ((col >> 3) & 3) * 16 + li) * 16) : 0x80000000u;
    PIN(hoff[j]);
  }
  u2 xva, xvb;

  f4 acc[2][3], cs[3], rq = {0.f, 0.f, 0.f, 0.f};
#pragma unroll
  for (int j = 0; j < 3; ++j) { acc[0][j] = acc[1][j] = cs[j] = (f4){0.f, 0.f, 0.f, 0.f}; }
  const b8 ones = {(__bf16)1.0f, (__bf16)1.0f, (__bf16)1.0f, (__bf16)1.0f, (__bf16)1.0f, (__bf16)1.0f, (__bf16)1.0f, (__bf16)1.0f};
  b8 xf[2][2], wf[2][3];
  auto rd = [&](int buf, int so) {
#pragma unroll
    for (int j = 0; j < 3; ++j) wf[buf][j] = cat(trread(wa[j] + so), trread(wa[j] + so + 16 * 384));
    xf[buf][0] = cat(*(const b4*)(xa00 + so), *(const b4*)(xa01 + so));
    xf[buf][1] = cat(*(const b4*)(xa10 + so), *(const b4*)(xa11 + so));
  };
  wait_vm<2 * (G1_S - 1)>();
  bar();
  rd(0, 0);
  sfor<0, 16>([&](auto K) {
    constexpr int k = K.v, u = k & 1;
    if constexpr (k < 15) {
      constexpr int later = ((k + G1_S) < 16 ? (k + G1_S) : 16) - (k + 2);
      wait_vm<2 * later + (k > 11 ? 2 : 0)>();
      bar();
      if constexpr (k + G1_S < 16) issue(k % G1_S, 0);
      if constexpr (k == 11) {
        xva = *(const u2*)(x + (size_t)(r0 + 2 * pi) * SD + 64 * pn + 4 * c4);
        xvb = *(const u2*)(x + (size_t)(r0 + 2 * pi + 1) * SD + 64 * pn + 4 * c4);
      }
      rd(u ^ 1, ((k + 1) % G1_S) * G1_STAGE);
    }
    __builtin_amdgcn_sched_barrier(0);
#pragma unroll
    for (int j = 0; j < 3; ++j) acc[0][j] = __builtin_amdgcn_mfma_f32_16x16x32_bf16(wf[u][j], xf[u][0], acc[0][j], 0, 0, 0);
    rq = __builtin_amdgcn_mfma_f32_16x16x32_bf16(xf[u][0], xf[u][0], rq, 0, 0, 0);
    __builtin_amdgcn_sched_barrier(0);
    if constexpr (k + G1_S < 16) issue(k % G1_S, 1);
    __builtin_amdgcn_sched_barrier(0);
#pragma unroll
    for (int j = 0; j < 3; ++j) acc[1][j] = __builtin_amdgcn_mfma_f32_16x16x32_bf16(wf[u][j], xf[u][1], acc[1][j], 0, 0, 0);
    if constexpr (u == WM) {
#pragma unroll
      for (int j = 0; j < 3; ++j) cs[j] = __builtin_amdgcn_mfma_f32_16x16x32_bf16(wf[u][j], ones, cs[j], 0, 0, 0);
    }
    __builtin_amdgcn_sched_barrier(0);
  });
  STAMP(xnt, 4);
  PIN(xva); PIN(xvb);
  float* raw = (float*)(sm + 2 * G1_STAGE);
  float2* stats = (float2*)(sm + 2 * G1_STAGE + 1024);
  float* csum = (float*)(sm + 2 * G1_STAGE + 2048);
  if (wn == 3 && g == 0) { raw[16 * mt0 + li] = acc[0][2][0]; raw[16 * mt1 + li] = acc[1][2][0]; }
  if (wn < 2 && g == q) raw[64 + 16 * mt0 + li] = p == 0 ? rq[0] : (p == 1 ? rq[1] : (p == 2 ? rq[2] : rq[3]));
  if (li == 0) {
#pragma unroll
    for (int j = 0; j < 3; ++j) *(f4*)(csum + WM * 192 + 16 * (3 * wn + j) + 4 * g) = cs[j];
  }
  lbar();
  if (tid < 64) {
    const float mean = raw[tid] * (1.0f / SD);
    float var = raw[64 + tid] * (1.0f / SD) - mean * mean; var = var < 0.f ? 0.f : var;
    stats[tid] = make_float2(bfr(mean), bfr(__builtin_amdgcn_sqrtf(bfr(bfr(var) + 1e-5f))));
  }
  f4 csr[3];
#pragma unroll
  for (int j = 0; j < 3; ++j)
    csr[j] = *(const f4*)(csum + 16 * (3 * wn + j) + 4 * g) + *(const f4*)(csum + 192 + 16 * (3 * wn + j) + 4 * g);
  lbar();
  unsigned* T = (unsigned*)(sm + 3 * G1_STAGE);
  {
    const float2 s0 = stats[2 * pi], s1 = stats[2 * pi + 1];
    const float i0 = __builtin_amdgcn_rcpf(s0.y), i1 = __builtin_amdgcn_rcpf(s1.y);
#pragma unroll
    for (int e = 0; e < 4; ++e) {
      const unsigned wva = xva[e >> 1], wvb = xvb[e >> 1];
      const float a = (e & 1) ? hi_bf(wva) : lo_bf(wva), b = (e & 1) ? hi_bf(wvb) : lo_bf(wvb);
      const float na = bfr(bfr(a - s0.x) * i0), nb = bfr(bfr(b - s1.x) * i1);
      T[(4 * c4 + e) * 32 + (pi ^ ((c4 >> 1) << 2))] = pk_h(na, nb);
    }
  }
  {
    const float2 st0 = stats[16 * mt0 + li], st1 = stats[16 * mt1 + li];
    const float inv0 = __builtin_amdgcn_rcpf(st0.y), inv1 = __builtin_amdgcn_rcpf(st1.y);
#pragma unroll
    for (int j = 0; j < 3; ++j) {
      float v[4], u[4];
#pragma unroll
      for (int r = 0; r < 4; ++r) {
        v[r] = fmaxf((acc[0][j][r] - st0.x * csr[j][r]) * inv0, 0.f);
        u[r] = fmaxf((acc[1][j][r] - st1.x * csr[j][r]) * inv1, 0.f);
      }
      u4 o = pair16((u2){pk_bf(v[0], v[1]), pk_bf(v[2], v[3])}, (u2){pk_bf(u[0], u[1]), pk_bf(u[2], u[3])});
      if (c0 + 16 * (3 * wn + j) >= SF) o = (u4){0u, 0u, 0u, 0u};
      st16(h, 128 * NKP * 1024, hoff[j], o);
    }
  }
  {
    lbar();
    const int ch = tid >> 3, m = tid & 7;
    const u4 v = *(const u4*)(T + ch * 32 + 4 * (m ^ (ch >> 3)));
    st16(xnt, SD * SL * 2, ((64 * pn + ch) * SL + r0 + 8 * m) * 2, v);
  }
}

DEVI void g1_role(int bid, char* sm, const u16* __restrict__ x, const u16* __restrict__ w1,
                  u16* __restrict__ h, _Float16* __restrict__ xnt) {
  const int w = __builtin_amdgcn_readfirstlane(threadIdx.x >> 6);
  __builtin_amdgcn_s_setprio(2);
  if (w < 4) g1_body<0>(bid, sm, x, w1, h, xnt, w); else g1_body<1>(bid, sm, x, w1, h, xnt, w);
}

constexpr int KG_CH = 80 * 512;
constexpr int KG_LDS = 2 * KG_CH;

struct cf { float r, i; };
DEVI cf cmul(cf a, cf b) {
  float t, u, re, im;
  asm("v_mul_f32 %0, %2, %3\n\tv_mul_f32 %1, %2, %4" : "=&v"(t), "=&v"(u) : "v"(a.i), "v"(b.i), "v"(b.r));
  asm("v_fma_f32 %0, %2, %3, -%4\n\tv_fma_f32 %1, %2, %5, %6" : "=&v"(re), "=&v"(im) : "v"(a.r), "v"(b.r), "v"(t), "v"(b.i), "v"(u));
  return {re, im};
}
DEVI cf cseed(float al, float om, float m) {
  const float mag = __builtin_amdgcn_exp2f((al * m) * 1.44269504088896341f);
  const float ph = om * m;
  const float chi = 0.15915494f, clo = 6.4206382e-9f;
  const float fr = __builtin_fmaf(ph, clo, __builtin_fmaf(ph, chi, -__builtin_rintf(ph * chi)));
  return {mag * __builtin_amdgcn_cosf(fr), mag * __builtin_amdgcn_sinf(fr)};
}

DEVI void kg_role(int kb, char* sm0, const u16* __restrict__ Are, const u16* __restrict__ Aim,
                  const u16* __restrict__ Bre, const u16* __restrict__ Bim, const u16* __restrict__ Cre,
                  const u16* __restrict__ Cim, const u16* __restrict__ logdt, const u16* __restrict__ w2,
                  _Float16* __restrict__ krev, float* __restrict__ ksc, u16* __restrict__ w2f) {
  const int l = threadIdx.x & 63;
  const int w8 = __builtin_amdgcn_readfirstlane(threadIdx.x >> 6);
  const int w = w8 & 3, tid = threadIdx.x & 255;
  const int d = 2 * kb + (w8 >> 2);
  char* sm = sm0 + (w8 >> 2) * KG_CH;
  const int n = tid & 127;
  const u16 ldt = logdt[d], are = Are[d * SN + n], aim = Aim[d * SN + n];
  const int fr = kb * 8 + w8;
  u16 wv[8] = {0, 0, 0, 0, 0, 0, 0, 0};
  if (fr < NKS * 32) {
    const u16* src = w2 + (size_t)(32 * (fr >> 5) + 8 * (l >> 4)) * SD + 16 * (fr & 31) + (l & 15);
#pragma unroll
    for (int e = 0; e < 8; ++e) wv[e] = src[(size_t)e * SD];
  }
  int xr, yr;
  {
    const int li = l & 15, y = li << w;
    yr = y < 16 ? y : (y < 32 ? 8 + (y >> 1) : (y < 64 ? 16 + (y >> 2) : 24 + (y >> 3)));
    xr = 40 + (w == 0 ? li : (w == 3 ? 32 + (li & 7) : 16 + li));
  }
  const int xbase = xr * 512 + (((l >> 4) ^ (xr & 15)) << 4), ybase = yr * 512 + (((l >> 4) ^ (yr & 15)) << 4);
  int ebase; unsigned epat;
  {
    const int m = 255 - tid;
    if (m < 32) { ebase = 8 * m; epat = 0x76543210u; }
    else if (m < 64) { ebase = 128 + 4 * m; epat = 0x43222100u; }
    else if (m < 128) { ebase = 256 + 2 * m; epat = 0x22111000u; }
    else { ebase = 384 + m; epat = (m & 1) ? 0x11110000u : 0x11100000u; }
  }
  const int kbase = w == 0 ? 0 : (w == 3 ? 512 : 256), ilo = w == 2 ? 8 : 0, ihi = (w & 1) ? 8 : 16;
  const float dt = bfr(expf(bf2f(ldt)));
  const float al = dt * bf2f(are), om = dt * bf2f(aim);
  const int wa = (n >> 2) * 16 + (n & 3) * 4;
#define KG_ST(row, re, im) *(unsigned*)(sm + (row) * 512 + (wa ^ (((row) & 15) << 4))) = pk_h(re, im)
  float k2048 = 0.f;
  if (w < 2) {
    cf P[16], Q[8], R[8];
    const cf e1 = cseed(al, om, 1.f), e16 = cseed(al, om, 16.f), e32 = cseed(al, om, 32.f), e64 = cseed(al, om, 64.f);
    P[0] = {1.f, 0.f}; P[1] = e1;
#pragma unroll
    for (int j = 2; j < 16; ++j) P[j] = cmul(P[j >> 1], P[j - (j >> 1)]);
#pragma unroll
    for (int j = 0; j < 16; ++j) KG_ST(j, P[j].r, P[j].i);
    Q[0] = e16;
#pragma unroll
    for (int m = 1; m < 8; ++m) Q[m] = cmul(e16, P[2 * m]);
#pragma unroll
    for (int m = 0; m < 8; ++m) KG_ST(16 + m, Q[m].r, Q[m].i);
    R[0] = e32;
#pragma unroll
    for (int m = 1; m < 8; ++m) R[m] = cmul(e32, m < 4 ? P[4 * m] : Q[2 * (m - 4)]);
#pragma unroll
    for (int m = 0; m < 8; ++m) KG_ST(24 + m, R[m].r, R[m].i);
    KG_ST(32, e64.r, e64.i);
#pragma unroll
    for (int m = 1; m < 8; ++m) {
      const cf t = cmul(e64, m < 2 ? P[8 * m] : (m < 4 ? Q[4 * (m - 2)] : R[2 * (m - 4)]));
      KG_ST(32 + m, t.r, t.i);
    }
  } else {
    cf cb[2];
    float mx = 0.f;
#pragma unroll
    for (int hh = 0; hh < 2; ++hh) {
      const int nn = (n & 63) + 64 * hh;
      const float br = dt * bf2f(Bre[d * SN + nn]), bi = dt * bf2f(Bim[d * SN + nn]);
      const float cr = bf2f(Cre[d * SN + nn]), ci = bf2f(Cim[d * SN + nn]);
      cb[hh] = cmul({cr, ci}, {br, bi});
      mx = fmaxf(mx, fmaxf(fabsf(cb[hh].r), fabsf(cb[hh].i)));
    }
    const float on = -om;
    const cf e16 = cseed(al, on, 16.f), e32 = cseed(al, on, 32.f), e64 = cseed(al, on, 64.f), e128 = cseed(al, on, 128.f);
    const cf e256 = cseed(al, on, 256.f), e512 = cseed(al, on, 512.f), e1024 = cseed(al, on, 1024.f);
    mx = wave_max(mx);
    int ex = (mx > 0.f && mx < 3.0e38f) ? __builtin_amdgcn_frexp_expf(mx) : 0;
    ex = ex < -100 ? -100 : (ex > 100 ? 100 : ex);
    const float sc = __builtin_amdgcn_ldexpf(1.0f, -ex);
    if (tid == 128) ksc[d] = __builtin_amdgcn_ldexpf(1.0f, ex);
    const cf c0 = (w == 2) ? cb[0] : cb[1];
    const cf cbs = {c0.r * sc, -c0.i * sc};
    cf a0 = cbs, a0b = cmul(cbs, e128), a1 = cmul(cbs, e256), a2 = cmul(cbs, e512), a3 = cmul(cbs, e1024);
#pragma unroll
    for (int i = 0; i < 8; ++i) {
      KG_ST(40 + i, a0.r, a0.i);
      KG_ST(48 + i, a0b.r, a0b.i);
      KG_ST(56 + i, a1.r, a1.i);
      KG_ST(64 + i, a2.r, a2.i);
      KG_ST(72 + i, a3.r, a3.i);
      if (i < 7) { a0 = cmul(a0, e16); a0b = cmul(a0b, e16); a1 = cmul(a1, e32); a2 = cmul(a2, e64); a3 = cmul(a3, e128); }
    }
    k2048 = wave_sum(a3.r * e128.r - a3.i * e128.i);
  }
#undef KG_ST
  if (fr < (NKS + 1) * 32) {
    const u4 f = {(unsigned)wv[0] | ((unsigned)wv[1] << 16), (unsigned)wv[2] | ((unsigned)wv[3] << 16),
                  (unsigned)wv[4] | ((unsigned)wv[5] << 16), (unsigned)wv[6] | ((unsigned)wv[7] << 16)};
    st16(w2f, (NKS + 1) * 32768, (fr * 64 + l) * 16, f);
  }
  STAMP((char*)krev - WS_KREV, 4);
  lbar();
  STAMP((char*)krev - WS_KREV, 5);
  {
    const int g = l >> 4;
    h8 xf[8], yf[8];
#pragma unroll
    for (int k4 = 0; k4 < 4; ++k4) {
      const char* xa = sm + (xbase ^ (k4 << 6));
      const char* ya = sm + (ybase ^ (k4 << 6));
      xf[k4] = *(const h8*)xa; xf[4 + k4] = *(const h8*)(xa + 256);
      yf[k4] = *(const h8*)ya; yf[4 + k4] = *(const h8*)(ya + 256);
    }
    lbar();
    f4 acc0 = {0.f, 0.f, 0.f, 0.f}, acc1 = {0.f, 0.f, 0.f, 0.f};
#pragma unroll
    for (int kk = 0; kk < 4; ++kk) {
      acc0 = __builtin_amdgcn_mfma_f32_16x16x32_f16(xf[2 * kk], yf[2 * kk], acc0, 0, 0, 0);
      acc1 = __builtin_amdgcn_mfma_f32_16x16x32_f16(xf[2 * kk + 1], yf[2 * kk + 1], acc1, 0, 0, 0);
    }
    _Float16* Kc = (_Float16*)sm;
#pragma unroll
    for (int r = 0; r < 4; ++r) {
      const int i = 4 * g + r;
      const bool ok = (i >= ilo) && (i < ihi);
      Kc[ok ? kbase + 16 * i + (l & 15) : 648] = (_Float16)(acc0[r] + acc1[r]);
    }
    if (w >= 2 && l == 0) ((float*)(sm + 1536))[w - 2] = k2048;
  }
  lbar();
  STAMP((char*)krev - WS_KREV, 6);
  {
    const _Float16* Kc = (const _Float16*)sm + ebase;
    h8 o;
#pragma unroll
    for (int e = 0; e < 8; ++e) o[e] = Kc[(epat >> (4 * (7 - e))) & 15];
    if (tid == 0) {
      const float* kp = (const float*)(sm + 1536);
      const _Float16 klast = (_Float16)(kp[0] + kp[1]);
      o[0] = klast; o[1] = klast; o[2] = klast; o[3] = klast;
    }
    st16(krev, SD * KSTR * 2, (d * KSTR + 8 * tid) * 2, __builtin_bit_cast(u4, o));
    if (tid < 8) st16(krev, SD * KSTR * 2, (d * KSTR + 2048 + 8 * tid) * 2, (u4){0u, 0u, 0u, 0u});
  }
}

#define CVP 1
constexpr int CV_KC = 4160;
constexpr int CV_U = 8 * CV_KC;
constexpr int CV_CH = CV_U + 95 * 80;
constexpr int CV_LDS = CV_CH;

DEVI void cv_role(int d, char* sm, const _Float16* __restrict__ xnt, const _Float16* __restrict__ krev,
                  const float* __restrict__ ksc, const u16* __restrict__ dskip, u16* __restrict__ at) {
  __builtin_amdgcn_s_setprio(3);
  const int l = threadIdx.x & 63;
  const int w = __builtin_amdgcn_readfirstlane(threadIdx.x >> 6);
  const int tid = threadIdx.x;
  const u4 uv = *(const u4*)(xnt + (size_t)d * SL + 8 * tid);
  const u4* kg = (const u4*)(krev + (size_t)d * KSTR);
  const u4 k0 = kg[tid], k1 = kg[tid + 1];
  u4 k2 = {0u, 0u, 0u, 0u}, k3 = {0u, 0u, 0u, 0u};
  if (tid < 3) { k2 = kg[256 + tid]; k3 = kg[257 + tid]; }
  const float scale = ksc[d];
  const float dsk = bf2f(dskip[d]);
  if (tid < 155) *(u4*)(sm + CV_U + tid * 16) = (u4){0u, 0u, 0u, 0u};
  *(u4*)(sm + CV_U + (31 + (tid >> 2)) * 80 + (tid & 3) * 16) = uv;
  auto build = [&](u4 a, u4 b, int q) {
    const unsigned wv[8] = {a.x, a.y, a.z, a.w, b.x, b.y, b.z, b.w};
    unsigned f[7];
#pragma unroll
    for (int m = 0; m < 7; ++m) f[m] = __builtin_amdgcn_alignbit(wv[m + 1], wv[m], 16);
#pragma unroll
    for (int m = 0; m < 4; ++m) {
      *(u4*)(sm + (2 * m) * CV_KC + 16 * q) = (u4){wv[m], wv[m + 1], wv[m + 2], wv[m + 3]};
      *(u4*)(sm + (2 * m + 1) * CV_KC + 16 * q) = (u4){f[m], f[m + 1], f[m + 2], f[m + 3]};
    }
  };
  build(k0, k1, tid);
  if (tid < 3) build(k2, k3, 256 + tid);
  lbar();
  STAMP(xnt, 8);
  __builtin_amdgcn_s_setprio(CVP);
  const int ks = w & 1, hb = w >> 1;
  const int b = l & 31, hh = l >> 5;
  const char* abase = sm + (7 - (b & 7)) * CV_KC + 16 * (255 - 4 * (16 * hb + 47) - (b >> 3) + 2 * ks + hh);
  const char* bbase = sm + CV_U + (31 - 16 * hb - 15 + b) * 80 + 32 * ks + 16 * hh;
  f16v t0 = {}, t1 = {};
#pragma unroll
  for (int i = 0; i < 16; ++i) {
    const h8 a_lo = *(const h8*)(abase + 64 * (15 - i) + 2048);
    const h8 a_hi = *(const h8*)(abase + 64 * (15 - i));
    const h8 b_lo = *(const h8*)(bbase + 80 * (15 - i));
    const h8 b_hi = *(const h8*)(bbase + 80 * (15 - i) + 2560);
    t0 = __builtin_amdgcn_mfma_f32_32x32x16_f16(a_lo, b_lo, t0, 0, 0, 0);
    t1 = __builtin_amdgcn_mfma_f32_32x32x16_f16(a_lo, b_hi, t1, 0, 0, 0);
    t1 = __builtin_amdgcn_mfma_f32_32x32x16_f16(a_hi, b_lo, t1, 0, 0, 0);
  }
  __builtin_amdgcn_s_setprio(3);
  lbar();
  STAMP(xnt, 9);
  f4 own[2];
  auto grp = [&](int G, int hf) -> f4 {
    const f16v& t = (G >> 1) ? t1 : t0; const int o = 8 * (G & 1) + 4 * hf;
    return (f4){t[o], t[o + 1], t[o + 2], t[o + 3]};
  };
  sfor<0, 4>([&](auto W) {
    if (w == W.v) {
#pragma unroll
      for (int s = 1; s < 4; ++s)
#pragma unroll
        for (int hf = 0; hf < 2; ++hf)
          *(f4*)(sm + ((((W.v + s) & 3) * 3 + (3 - s)) * 2 + hf) * 1024 + l * 16) = grp((W.v + s) & 3, hf);
      own[0] = grp(W.v, 0); own[1] = grp(W.v, 1);
    }
  });
  const int arow = 32 * (w >> 1) + b;
  const h8 uu = *(const h8*)(sm + CV_U + (31 + arow) * 80 + 32 * (w & 1) + 16 * hh);
  lbar();
#pragma unroll
  for (int sI = 0; sI < 3; ++sI)
#pragma unroll
    for (int hf = 0; hf < 2; ++hf) own[hf] += *(const f4*)(sm + ((w * 3 + sI) * 2 + hf) * 1024 + l * 16);
  {
    unsigned y0 = pk_bf(own[0][0] * scale, own[0][1] * scale), y1 = pk_bf(own[0][2] * scale, own[0][3] * scale);
    unsigned y2 = pk_bf(own[1][0] * scale, own[1][1] * scale), y3 = pk_bf(own[1][2] * scale, own[1][3] * scale);
    const auto ra = __builtin_amdgcn_permlane32_swap(y0, y2, false, false);
    const auto rb = __builtin_amdgcn_permlane32_swap(y1, y3, false, false);
    const unsigned yy[4] = {ra[0], rb[0], ra[1], rb[1]};
    u4 ov;
#pragma unroll
    for (int e = 0; e < 4; ++e) {
      const unsigned v = pk_bf(lo_bf(yy[e]) + bfr((float)uu[2 * e] * dsk), hi_bf(yy[e]) + bfr((float)uu[2 * e + 1] * dsk));
      ov[e] = v == 0xffffffffu ? 0x7fc07fc0u : v;
    }
    st16(at, SD * SL * 2, (d * SL + 32 * arow + 16 * (w & 1) + 8 * hh) * 2, ov);
  }
  STAMP(xnt, 10);
}

constexpr int AUX_SC1_VOL = (int)0x80000010u;
constexpr int G2_LDS = 49152;
#define G2_NB 3

DEVI void g2_role(int bid, char* sm, const u16* __restrict__ x, const u16* __restrict__ hfr,
                  const u16* __restrict__ w2f, u16* at, u16* __restrict__ out) {
  __builtin_amdgcn_s_setprio(1);
  const int tid = threadIdx.x, l = tid & 63;
  const int w = __builtin_amdgcn_readfirstlane(tid >> 6);
  const int xcd = bid & 7, idx = bid >> 3;
  const int pm = (xcd >> 1) * 8 + (idx >> 2), pn = (xcd & 1) * 4 + (idx & 3);
  const int t0 = pm * 64, d0 = pn * 64;
  const int g = l >> 4, li = l & 15, q = li >> 2, p = l & 3;
  const int k0 = 11 * w;

  const __amdgpu_buffer_rsrc_t rh = __builtin_amdgcn_make_buffer_rsrc((void*)hfr, 0, 128 * NKP * 1024, 0x00020000);
  const __amdgpu_buffer_rsrc_t rw = __builtin_amdgcn_make_buffer_rsrc((void*)w2f, 0, NKP * 32768, 0x00020000);
  const int lo = l * 16;
  int hso[4];
#pragma unroll
  for (int s = 0; s < 4; ++s) hso[s] = ((pm * 4 + ((w + s) & 3)) * NKP + k0) * 1024;
  const int wso = (k0 * 32 + pn * 4) * 1024;
  u4 hf[G2_NB][4], wf[G2_NB][4];
  auto ldw = [&](int buf, int i) {
#pragma unroll
    for (int j = 0; j < 4; ++j) wf[buf][j] = __builtin_amdgcn_raw_buffer_load_b128(rw, lo + j * 1024, wso + i * 32768, 0);
  };
  auto ldh = [&](int buf, int i, int s) { hf[buf][s] = __builtin_amdgcn_raw_buffer_load_b128(rh, lo, hso[s] + i * 1024, 0); };
  f4 acc[4][4];
#pragma unroll
  for (int j = 0; j < 4; ++j)
#pragma unroll
    for (int s = 0; s < 4; ++s) acc[j][s] = (f4){0.f, 0.f, 0.f, 0.f};
#pragma unroll
  for (int i = 0; i < G2_NB; ++i) {
    ldw(i, i);
#pragma unroll
    for (int s = 0; s < 4; ++s) ldh(i, i, s);
    __builtin_amdgcn_sched_barrier(0);
  }
  u2 xr[4];
  const __amdgpu_buffer_rsrc_t rs = __builtin_amdgcn_make_buffer_rsrc((void*)at, 0, SD * SL * 2, 0x00020000);
  sfor<0, 11>([&](auto I) {
    constexpr int i = I.v, b = i % G2_NB;
#pragma unroll
    for (int s = 0; s < 4; ++s) {
#pragma unroll
      for (int j = 0; j < 4; ++j)
        acc[j][s] = __builtin_amdgcn_mfma_f32_16x16x32_bf16(__builtin_bit_cast(b8, wf[b][j]), __builtin_bit_cast(b8, hf[b][s]), acc[j][s], 0, 0, 0);
      __builtin_amdgcn_sched_barrier(0);
      if constexpr (i + G2_NB < 11) { ldh(b, i + G2_NB, s); __builtin_amdgcn_sched_barrier(0); }
    }
    if constexpr (i + G2_NB < 11) { ldw(b, i + G2_NB); __builtin_amdgcn_sched_barrier(0); }
    if constexpr (i == 8) {
#pragma unroll
      for (int j = 0; j < 4; ++j) xr[j] = *(const u2*)(x + (size_t)(t0 + 16 * w + li) * SD + d0 + 16 * j + 4 * g);
      __builtin_amdgcn_sched_barrier(0);
    }
  });
#pragma unroll
  for (int s = 1; s < 4; ++s)
#pragma unroll
    for (int j = 0; j < 4; ++j) *(f4*)(sm + ((((w + s) & 3) * 3 + (3 - s)) * 4 + j) * 1024 + l * 16) = acc[j][s];
  STAMP((char*)hfr - WS_H, 8);
  const int aoff = ((d0 + l) * SL + t0 + 16 * w) * 2;
  u4 av0 = __builtin_amdgcn_raw_buffer_load_b128(rs, aoff, 0, AUX_SC1_VOL);
  u4 av1 = __builtin_amdgcn_raw_buffer_load_b128(rs, aoff + 16, 0, AUX_SC1_VOL);
  lbar();
  f4 v[4];
#pragma unroll
  for (int j = 0; j < 4; ++j) {
    v[j] = acc[j][0];
#pragma unroll
    for (int sI = 0; sI < 3; ++sI) v[j] += *(const f4*)(sm + ((w * 3 + sI) * 4 + j) * 1024 + l * 16);
    PIN(v[j]);
  }
  for (unsigned spins = 0;; ++spins) {
    const bool miss = (av0.x == 0xffffffffu) | (av0.y == 0xffffffffu) | (av0.z == 0xffffffffu) | (av0.w == 0xffffffffu) |
                      (av1.x == 0xffffffffu) | (av1.y == 0xffffffffu) | (av1.z == 0xffffffffu) | (av1.w == 0xffffffffu);
    if (!__any(miss) || spins > (1u << 18)) break;
    __builtin_amdgcn_s_sleep(12);
    if (miss) {
      av0 = __builtin_amdgcn_raw_buffer_load_b128(rs, aoff, 0, AUX_SC1_VOL);
      av1 = __builtin_amdgcn_raw_buffer_load_b128(rs, aoff + 16, 0, AUX_SC1_VOL);
    }
  }
  STAMP((char*)hfr - WS_H, 9);
  asm volatile("s_waitcnt lgkmcnt(0)" ::: "memory");
  char* myR = sm + w * 12288;
  *(u4*)(myR + l * 32) = av0;
  *(u4*)(myR + l * 32 + 16) = av1;
  asm volatile("s_waitcnt lgkmcnt(0)" ::: "memory");
  STAMP((char*)hfr - WS_H, 10);
  u2 ov[4];
#pragma unroll
  for (int j = 0; j < 4; ++j) {
    const b4 a4 = trread(myR + (16 * j + 4 * g + q) * 32 + 8 * p);
    const u2 xv = xr[j];
    const float xs[4] = {lo_bf(xv.x), hi_bf(xv.x), lo_bf(xv.y), hi_bf(xv.y)};
    float o[4];
#pragma unroll
    for (int r = 0; r < 4; ++r) o[r] = bfr(xs[r] + (float)a4[r]) + bfr(v[j][r]);
    ov[j] = (u2){pk_bf(o[0], o[1]), pk_bf(o[2], o[3])};
  }
  const unsigned oo = ((t0 + 16 * w + li) * SD + d0 + 16 * (g & 1) + 8 * (g >> 1)) * 2;
  st16(out, SL * SD * 2, oo, pair16(ov[0], ov[1]));
  st16(out, SL * SD * 2, oo + 64, pair16(ov[2], ov[3]));
}

constexpr int P1_LDS = G1_LDS > KG_LDS ? G1_LDS : KG_LDS;
constexpr int P2_LDS = G2_LDS > CV_LDS ? G2_LDS : CV_LDS;

__global__ __launch_bounds__(512, 4) void k_phase1(const u16* x, const u16* Are, const u16* Aim, const u16* Bre,
                                                   const u16* Bim, const u16* Cre, const u16* Cim, const u16* logdt,
                                                   const u16* w1, const u16* w2, char* ws, int boff) {
  extern __shared__ __attribute__((aligned(16))) char sm[];
  const int bid = blockIdx.x + boff;
  STAMP(ws, 0);
  if (bid < 256) {
    g1_role(bid, sm, x, w1, (u16*)(ws + WS_H), (_Float16*)(ws + WS_XNT));
  } else {
    st16(ws + WS_AT, SD * SL * 2, ((bid - 256) * 512 + threadIdx.x) * 16, (u4){0xffffffffu, 0xffffffffu, 0xffffffffu, 0xffffffffu});
    kg_role(bid - 256, sm, Are, Aim, Bre, Bim, Cre, Cim, logdt, w2, (_Float16*)(ws + WS_KREV), (float*)(ws + WS_KSC),
            (u16*)(ws + WS_W2F));
  }
  STAMP(ws, 1);
}

__global__ __launch_bounds__(256, 3) void k_phase2(const u16* x, const u16* dskip, char* ws, u16* out, int boff) {
  extern __shared__ __attribute__((aligned(16))) char sm[];
  const int bid = blockIdx.x + boff;
  STAMP(ws, 2);
  if (bid >= 512) {
    g2_role(bid - 512, sm, x, (const u16*)(ws + WS_H), (const u16*)(ws + WS_W2F), (u16*)(ws + WS_AT), out);
  } else {
    cv_role(bid, sm, (const _Float16*)(ws + WS_XNT), (const _Float16*)(ws + WS_KREV), (const float*)(ws + WS_KSC),
            dskip, (u16*)(ws + WS_AT));
  }
  STAMP(ws, 3);
}

static void launch_p1(void* const* d_in, void* d_ws, hipStream_t stream, int grid, int boff) {
  (void)hipFuncSetAttribute((const void*)k_phase1, hipFuncAttributeMaxDynamicSharedMemorySize, P1_LDS);
  k_phase1<<<grid, 512, P1_LDS, stream>>>((const u16*)d_in[0], (const u16*)d_in[1], (const u16*)d_in[2], (const u16*)d_in[3],
                                          (const u16*)d_in[4], (const u16*)d_in[5], (const u16*)d_in[6],
                                          (const u16*)d_in[7], (const u16*)d_in[10], (const u16*)d_in[11], (char*)d_ws, boff);
}
static void launch_p2(void* const* d_in, void* d_out, void* d_ws, hipStream_t stream, int grid, int boff) {
  (void)hipFuncSetAttribute((const void*)k_phase2, hipFuncAttributeMaxDynamicSharedMemorySize, P2_LDS);
  k_phase2<<<grid, 256, P2_LDS, stream>>>((const u16*)d_in[0], (const u16*)d_in[8], (char*)d_ws, (u16*)d_out, boff);
}

extern "C" void kernel_launch(void* const* d_in, const int* in_sizes, int n_in, void* d_out, int out_size,
                              void* d_ws, size_t ws_size, hipStream_t stream) {
  launch_p1(d_in, d_ws, stream, 512, 0);
  launch_p2(d_in, d_out, d_ws, stream, 768, 0);
}
```

```cpp
#include <hip/hip_runtime.h>
#include <hip/hip_bf16.h>
#include <stdint.h>

typedef unsigned short u16;
typedef _Float16 h8 __attribute__((ext_vector_type(8)));
typedef __bf16 b8 __attribute__((ext_vector_type(8)));
typedef __bf16 b4 __attribute__((ext_vector_type(4)));
typedef __bf16 b2 __attribute__((ext_vector_type(2)));
typedef float f4 __attribute__((ext_vector_type(4)));
typedef float f16v __attribute__((ext_vector_type(16)));
typedef unsigned u4 __attribute__((ext_vector_type(4)));
typedef unsigned u2 __attribute__((ext_vector_type(2)));
#define LAS __attribute__((address_space(3)))
#define GAS __attribute__((address_space(1)))
#define DEVI __device__ __forceinline__

constexpr int SL = 2048, SD = 512, SN = 128, SF = 1376;
constexpr int KSTR = 2112;
constexpr int NKS = SF / 32;
constexpr int NKP = NKS + 1;
constexpr size_t WS_XNT = 0;
constexpr size_t WS_KREV = WS_XNT + (size_t)SD * SL * 2;
constexpr size_t WS_KSC = WS_KREV + (size_t)SD * KSTR * 2;
constexpr int CNT_STR = 1088;
constexpr size_t WS_CNT = WS_KSC + 4096;
constexpr size_t WS_H = WS_CNT + 64 * CNT_STR * 4;
constexpr size_t WS_AT = WS_H + (size_t)128 * NKP * 1024;
constexpr size_t WS_W2F = WS_AT + (size_t)SD * SL * 2;

#define STAMP(ws, slot) do {} while (0)
DEVI float bf2f(u16 v) { return __uint_as_float(((unsigned)v) << 16); }
DEVI float bfr(float f) { return (float)(__bf16)f; }
DEVI unsigned pk_bf(float a, float b) { b2 v = {(__bf16)a, (__bf16)b}; return __builtin_bit_cast(unsigned, v); }
DEVI unsigned pk_h(float a, float b) { unsigned r; asm("v_cvt_pk_f16_f32 %0, %1, %2" : "=v"(r) : "v"(a), "v"(b)); return r; }
DEVI float lo_bf(unsigned v) { return __uint_as_float(v << 16); }
DEVI float hi_bf(unsigned v) { return __uint_as_float(v & 0xffff0000u); }
template <int N> DEVI void wait_vm() { asm volatile("s_waitcnt vmcnt(%0) lgkmcnt(0)" ::"n"(N) : "memory"); }
DEVI void bar() { asm volatile("" ::: "memory"); __builtin_amdgcn_s_barrier(); asm volatile("" ::: "memory"); }
DEVI void lbar() { asm volatile("s_waitcnt lgkmcnt(0)" ::: "memory"); __builtin_amdgcn_s_barrier(); asm volatile("" ::: "memory"); }
DEVI void glds16(const void* g, char* l) {
  __builtin_amdgcn_global_load_lds((const GAS void*)g, (LAS void*)l, 16, 0, 0);
}
DEVI b4 trread(const char* p) { return __builtin_amdgcn_ds_read_tr16_b64_v4bf16((LAS b4*)p); }
DEVI b8 cat(b4 a, b4 b) { return __builtin_shufflevector(a, b, 0, 1, 2, 3, 4, 5, 6, 7); }
template <int V> struct IC { static constexpr int v = V; };
template <int I, int N, class F> DEVI void sfor(F&& f) { if constexpr (I < N) { f(IC<I>{}); sfor<I + 1, N>(f); } }
DEVI void st16(void* base, unsigned bytes, unsigned off, u4 v) {
  __builtin_amdgcn_raw_buffer_store_b128(v, __builtin_amdgcn_make_buffer_rsrc(base, 0, bytes, 0x00020000), off, 0, 16);
}
DEVI u4 pair16(u2 a, u2 b) {
  const auto rx = __builtin_amdgcn_permlane16_swap(a.x, b.x, false, false);
  const auto ry = __builtin_amdgcn_permlane16_swap(a.y, b.y, false, false);
  return (u4){rx[0], ry[0], rx[1], ry[1]};
}
template <int CTRL, int RM> DEVI float dppf(float v) {
  return __builtin_bit_cast(float, __builtin_amdgcn_update_dpp(0, __builtin_bit_cast(int, v), CTRL, RM, 0xf, false));
}
DEVI float wave_max(float v) {
  v = fmaxf(v, dppf<0x111, 0xf>(v)); v = fmaxf(v, dppf<0x112, 0xf>(v)); v = fmaxf(v, dppf<0x114, 0xf>(v));
  v = fmaxf(v, dppf<0x118, 0xf>(v)); v = fmaxf(v, dppf<0x142, 0xa>(v)); v = fmaxf(v, dppf<0x143, 0xc>(v));
  return __builtin_bit_cast(float, __builtin_amdgcn_readlane(__builtin_bit_cast(int, v), 63));
}
DEVI float wave_sum(float v) {
  v += dppf<0x111, 0xf>(v); v += dppf<0x112, 0xf>(v); v += dppf<0x114, 0xf>(v);
  v += dppf<0x118, 0xf>(v); v += dppf<0x142, 0xa>(v); v += dppf<0x143, 0xc>(v);
  return __builtin_bit_cast(float, __builtin_amdgcn_readlane(__builtin_bit_cast(int, v), 63));
}
#define PIN(x) asm volatile("" : "+v"(x))
__device__ const unsigned g_ones[4] = {0x3f803f80u, 0x3f803f80u, 0x3f803f80u, 0x3f803f80u};

constexpr int G1_S = 5;
constexpr int G1_STAGE = 16384;
constexpr int G1_LDS = G1_S * G1_STAGE;

template <int WM>
DEVI void g1_body(int bid, char* sm, const u16* __restrict__ x, const u16* __restrict__ w1,
                  u16* __restrict__ h, _Float16* __restrict__ xnt, int w) {
  const int tid = threadIdx.x, l = tid & 63;
  const int wn = w & 3;
  const int xcd = bid & 7, idx = bid >> 3;
  const int pm = (xcd >> 1) * 8 + (idx >> 2), pn = (xcd & 1) * 4 + (idx & 3);
  const int r0 = pm * 64, c0 = pn * 176;
  const int g = l >> 4, li = l & 15, q = li >> 2, p = l & 3;

  const char* ga[2];
  unsigned gs[2];
#pragma unroll
  for (int j = 0; j < 2; ++j) {
    const int pc = 2 * w + j;
    if (pc < 4) {
      const int xrow = 16 * pc + (l >> 2);
      ga[j] = (const char*)(x + (size_t)(r0 + xrow) * SD + (((l & 3) ^ ((xrow >> 2) & 3)) * 8));
      gs[j] = 64;
    } else {
      const int ci = (pc - 4) * 64 + l, k = ci / 24, cp = ci - k * 24;
      const int c = cp ^ (((k >> 1) & 3) << 1);
      int col = c0 + 8 * c; if (col > SF - 8) col = SF - 8;
      ga[j] = c >= 22 ? (const char*)g_ones : (const char*)(w1 + (size_t)k * SF + col);
      gs[j] = c >= 22 ? 0 : 32 * SF * 2;
    }
  }
  char* const lb = sm + w * 2048;
  unsigned kc = (2 * (idx >> 2) + 4 * (idx & 3)) & 15;
  auto issue = [&](int slot, int j) {
    glds16(ga[j] + (unsigned long)kc * gs[j], lb + slot * G1_STAGE + j * 1024);
    if (j == 1) kc = (kc + 1) & 15;
  };
#pragma unroll
  for (int s = 0; s < G1_S; ++s) { issue(s, 0); issue(s, 1); }

  const int mt0 = 2 * WM + (wn & 1), mt1 = 2 * WM + ((wn & 1) ^ 1);
  const int xo0 = li * 64 + (((g >> 1) ^ q) * 16) + 8 * (g & 1), xo1 = li * 64 + (((2 + (g >> 1)) ^ q) * 16) + 8 * (g & 1);
  const char* xa00 = sm + mt0 * 1024 + xo0; const char* xa01 = sm + mt0 * 1024 + xo1;
  const char* xa10 = sm + mt1 * 1024 + xo0; const char* xa11 = sm + mt1 * 1024 + xo1;
  const char* wa[3];
#pragma unroll
  for (int j = 0; j < 3; ++j) {
    const int row = 4 * g + q;
    wa[j] = sm + 4096 + row * 384 + (((3 * wn + j) ^ ((row >> 1) & 3)) * 32) + 8 * p;
  }
  const int pi = tid >> 4, c4 = tid & 15;
  unsigned hoff[3];
#pragma unroll
  for (int j = 0; j < 3; ++j) {
    const int col = c0 + 16 * (3 * wn + j) + 8 * (g >> 1);
    const bool ok = (3 * wn + j < 11);
    hoff[j] = ok ? (unsigned)((((pm * 4 + ((g & 1) ? mt1 : mt0)) * NKP + (col >> 5)) * 64 + ((col >> 3) & 3) * 16 + li) * 16) : 0x80000000u;
    PIN(hoff[j]);
  }
  u2 xva, xvb;

  f4 acc[2][3], cs[3], rq = {0.f, 0.f, 0.f, 0.f};
#pragma unroll
  for (int j = 0; j < 3; ++j) { acc[0][j] = acc[1][j] = cs[j] = (f4){0.f, 0.f, 0.f, 0.f}; }
  const b8 ones = {(__bf16)1.0f, (__bf16)1.0f, (__bf16)1.0f, (__bf16)1.0f, (__bf16)1.0f, (__bf16)1.0f, (__bf16)1.0f, (__bf16)1.0f};
  b8 xf[2][2], wf[2][3];
  auto rd = [&](int buf, int so) {
#pragma unroll
    for (int j = 0; j < 3; ++j) wf[buf][j] = cat(trread(wa[j] + so), trread(wa[j] + so + 16 * 384));
    xf[buf][0] = cat(*(const b4*)(xa00 + so), *(const b4*)(xa01 + so));
    xf[buf][1] = cat(*(const b4*)(xa10 + so), *(const b4*)(xa11 + so));
  };
  wait_vm<2 * (G1_S - 1)>();
  bar();
  rd(0, 0);
  sfor<0, 16>([&](auto K) {
    constexpr int k = K.v, u = k & 1;
    if constexpr (k < 15) {
      constexpr int later = ((k + G1_S) < 16 ? (k + G1_S) : 16) - (k + 2);
      wait_vm<2 * later + (k > 11 ? 2 : 0)>();
      bar();
      if constexpr (k + G1_S < 16) issue(k % G1_S, 0);
      if constexpr (k == 11) {
        xva = *(const u2*)(x + (size_t)(r0 + 2 * pi) * SD + 64 * pn + 4 * c4);
        xvb = *(const u2*)(x + (size_t)(r0 + 2 * pi + 1) * SD + 64 * pn + 4 * c4);
      }
      rd(u ^ 1, ((k + 1) % G1_S) * G1_STAGE);
    }
    __builtin_amdgcn_sched_barrier(0);
#pragma unroll
    for (int j = 0; j < 3; ++j) acc[0][j] = __builtin_amdgcn_mfma_f32_16x16x32_bf16(wf[u][j], xf[u][0], acc[0][j], 0, 0, 0);
    rq = __builtin_amdgcn_mfma_f32_16x16x32_bf16(xf[u][0], xf[u][0], rq, 0, 0, 0);
    __builtin_amdgcn_sched_barrier(0);
    if constexpr (k + G1_S < 16) issue(k % G1_S, 1);
    __builtin_amdgcn_sched_barrier(0);
#pragma unroll
    for (int j = 0; j < 3; ++j) acc[1][j] = __builtin_amdgcn_mfma_f32_16x16x32_bf16(wf[u][j], xf[u][1], acc[1][j], 0, 0, 0);
    if constexpr (u == WM) {
#pragma unroll
      for (int j = 0; j < 3; ++j) cs[j] = __builtin_amdgcn_mfma_f32_16x16x32_bf16(wf[u][j], ones, cs[j], 0, 0, 0);
    }
    __builtin_amdgcn_sched_barrier(0);
  });
  STAMP(xnt, 4);
  PIN(xva); PIN(xvb);
  float* raw = (float*)(sm + 2 * G1_STAGE);
  float2* stats = (float2*)(sm + 2 * G1_STAGE + 1024);
  float* csum = (float*)(sm + 2 * G1_STAGE + 2048);
  if (wn == 3 && g == 0) { raw[16 * mt0 + li] = acc[0][2][0]; raw[16 * mt1 + li] = acc[1][2][0]; }
  if (wn < 2 && g == q) raw[64 + 16 * mt0 + li] = p == 0 ? rq[0] : (p == 1 ? rq[1] : (p == 2 ? rq[2] : rq[3]));
  if (li == 0) {
#pragma unroll
    for (int j = 0; j < 3; ++j) *(f4*)(csum + WM * 192 + 16 * (3 * wn + j) + 4 * g) = cs[j];
  }
  lbar();
  if (tid < 64) {
    const float mean = raw[tid] * (1.0f / SD);
    float var = raw[64 + tid] * (1.0f / SD) - mean * mean; var = var < 0.f ? 0.f : var;
    stats[tid] = make_float2(bfr(mean), bfr(__builtin_amdgcn_sqrtf(bfr(bfr(var) + 1e-5f))));
  }
  f4 csr[3];
#pragma unroll
  for (int j = 0; j < 3; ++j)
    csr[j] = *(const f4*)(csum + 16 * (3 * wn + j) + 4 * g) + *(const f4*)(csum + 192 + 16 * (3 * wn + j) + 4 * g);
  lbar();
  unsigned* T = (unsigned*)(sm + 3 * G1_STAGE);
  {
    const float2 s0 = stats[2 * pi], s1 = stats[2 * pi + 1];
    const float i0 = __builtin_amdgcn_rcpf(s0.y), i1 = __builtin_amdgcn_rcpf(s1.y);
#pragma unroll
    for (int e = 0; e < 4; ++e) {
      const unsigned wva = xva[e >> 1], wvb = xvb[e >> 1];
      const float a = (e & 1) ? hi_bf(wva) : lo_bf(wva), b = (e & 1) ? hi_bf(wvb) : lo_bf(wvb);
      const float na = bfr(bfr(a - s0.x) * i0), nb = bfr(bfr(b - s1.x) * i1);
      T[(4 * c4 + e) * 32 + (pi ^ ((c4 >> 1) << 2))] = pk_h(na, nb);
    }
  }
  {
    const float2 st0 = stats[16 * mt0 + li], st1 = stats[16 * mt1 + li];
    const float inv0 = __builtin_amdgcn_rcpf(st0.y), inv1 = __builtin_amdgcn_rcpf(st1.y);
#pragma unroll
    for (int j = 0; j < 3; ++j) {
      float v[4], u[4];
#pragma unroll
      for (int r = 0; r < 4; ++r) {
        v[r] = fmaxf((acc[0][j][r] - st0.x * csr[j][r]) * inv0, 0.f);
        u[r] = fmaxf((acc[1][j][r] - st1.x * csr[j][r]) * inv1, 0.f);
      }
      u4 o = pair16((u2){pk_bf(v[0], v[1]), pk_bf(v[2], v[3])}, (u2){pk_bf(u[0], u[1]), pk_bf(u[2], u[3])});
      if (c0 + 16 * (3 * wn + j) >= SF) o = (u4){0u, 0u, 0u, 0u};
      st16(h, 128 * NKP * 1024, hoff[j], o);
    }
  }
  {
    lbar();
    const int ch = tid >> 3, m = tid & 7;
    const u4 v = *(const u4*)(T + ch * 32 + 4 * (m ^ (ch >> 3)));
    st16(xnt, SD * SL * 2, ((64 * pn + ch) * SL + r0 + 8 * m) * 2, v);
  }
}

DEVI void g1_role(int bid, char* sm, const u16* __restrict__ x, const u16* __restrict__ w1,
                  u16* __restrict__ h, _Float16* __restrict__ xnt) {
  const int w = __builtin_amdgcn_readfirstlane(threadIdx.x >> 6);
  __builtin_amdgcn_s_setprio(2);
  if (w < 4) g1_body<0>(bid, sm, x, w1, h, xnt, w); else g1_body<1>(bid, sm, x, w1, h, xnt, w);
}

#define KGP 0
constexpr int KG_CH = 80 * 512;
constexpr int KG_LDS = 2 * KG_CH;

struct cf { float r, i; };
DEVI cf cmul(cf a, cf b) {
  float t, u, re, im;
  asm("v_mul_f32 %0, %2, %3\n\tv_mul_f32 %1, %2, %4" : "=&v"(t), "=&v"(u) : "v"(a.i), "v"(b.i), "v"(b.r));
  asm("v_fma_f32 %0, %2, %3, -%4\n\tv_fma_f32 %1, %2, %5, %6" : "=&v"(re), "=&v"(im) : "v"(a.r), "v"(b.r), "v"(t), "v"(b.i), "v"(u));
  return {re, im};
}
DEVI cf cseed(float al, float om, float m) {
  const float mag = __builtin_amdgcn_exp2f((al * m) * 1.44269504088896341f);
  const float ph = om * m;
  const float chi = 0.15915494f, clo = 6.4206382e-9f;
  const float fr = __builtin_fmaf(ph, clo, __builtin_fmaf(ph, chi, -__builtin_rintf(ph * chi)));
  return {mag * __builtin_amdgcn_cosf(fr), mag * __builtin_amdgcn_sinf(fr)};
}

DEVI void kg_role(int kb, char* sm0, const u16* __restrict__ Are, const u16* __restrict__ Aim,
                  const u16* __restrict__ Bre, const u16* __restrict__ Bim, const u16* __restrict__ Cre,
                  const u16* __restrict__ Cim, const u16* __restrict__ logdt, const u16* __restrict__ w2,
                  _Float16* __restrict__ krev, float* __restrict__ ksc, u16* __restrict__ w2f, u16* __restrict__ at) {
  const int l = threadIdx.x & 63;
  const int w8 = __builtin_amdgcn_readfirstlane(threadIdx.x >> 6);
  const int w = w8 & 3, tid = threadIdx.x & 255;
  const int d = 2 * kb + (w8 >> 2);
  char* sm = sm0 + (w8 >> 2) * KG_CH;
  const int n = tid & 127;
  __builtin_amdgcn_s_setprio(3);
  const u16 ldt = logdt[d], are = Are[d * SN + n], aim = Aim[d * SN + n];
  u16 pbr[2], pbi[2], pcr[2], pci[2];
#pragma unroll
  for (int hh = 0; hh < 2; ++hh) {
    const int nn = d * SN + (n & 63) + 64 * hh;
    pbr[hh] = Bre[nn]; pbi[hh] = Bim[nn]; pcr[hh] = Cre[nn]; pci[hh] = Cim[nn];
  }
  __builtin_amdgcn_sched_barrier(0);
  const int fr = kb * 8 + w8;
  u16 wv[8] = {0, 0, 0, 0, 0, 0, 0, 0};
  if (fr < NKS * 32) {
    const u16* src = w2 + (size_t)(32 * (fr >> 5) + 8 * (l >> 4)) * SD + 16 * (fr & 31) + (l & 15);
#pragma unroll
    for (int e = 0; e < 8; ++e) wv[e] = src[(size_t)e * SD];
  }
  st16(at, SD * SL * 2, (kb * 512 + threadIdx.x) * 16, (u4){0xffffffffu, 0xffffffffu, 0xffffffffu, 0xffffffffu});
  __builtin_amdgcn_s_setprio(KGP);
  int xr, yr;
  {
    const int li = l & 15, y = li << w;
    yr = y < 16 ? y : (y < 32 ? 8 + (y >> 1) : (y < 64 ? 16 + (y >> 2) : 24 + (y >> 3)));
    xr = 40 + (w == 0 ? li : (w == 3 ? 32 + (li & 7) : 16 + li));
  }
  const int xbase = xr * 512 + (((l >> 4) ^ (xr & 15)) << 4), ybase = yr * 512 + (((l >> 4) ^ (yr & 15)) << 4);
  int ebase; unsigned epat;
  {
    const int m = 255 - tid;
    if (m < 32) { ebase = 8 * m; epat = 0x76543210u; }
    else if (m < 64) { ebase = 128 + 4 * m; epat = 0x43222100u; }
    else if (m < 128) { ebase = 256 + 2 * m; epat = 0x22111000u; }
    else { ebase = 384 + m; epat = (m & 1) ? 0x11110000u : 0x11100000u; }
  }
  const int kbase = w == 0 ? 0 : (w == 3 ? 512 : 256), ilo = w == 2 ? 8 : 0, ihi = (w & 1) ? 8 : 16;
  const float dt = bfr(expf(bf2f(ldt)));
  const float al = dt * bf2f(are), om = dt * bf2f(aim);
  const int wa = (n >> 2) * 16 + (n & 3) * 4;
#define KG_ST(row, re, im) *(unsigned*)(sm + (row) * 512 + (wa ^ (((row) & 15) << 4))) = pk_h(re, im)
  float k2048 = 0.f;
  if (w < 2) {
    cf P[16], Q[8], R[8];
    const cf e1 = cseed(al, om, 1.f), e16 = cseed(al, om, 16.f), e32 = cseed(al, om, 32.f), e64 = cseed(al, om, 64.f);
    P[0] = {1.f, 0.f}; P[1] = e1;
#pragma unroll
    for (int j = 2; j < 16; ++j) P[j] = cmul(P[j >> 1], P[j - (j >> 1)]);
#pragma unroll
    for (int j = 0; j < 16; ++j) KG_ST(j, P[j].r, P[j].i);
    Q[0] = e16;
#pragma unroll
    for (int m = 1; m < 8; ++m) Q[m] = cmul(e16, P[2 * m]);
#pragma unroll
    for (int m = 0; m < 8; ++m) KG_ST(16 + m, Q[m].r, Q[m].i);
    R[0] = e32;
#pragma unroll
    for (int m = 1; m < 8; ++m) R[m] = cmul(e32, m < 4 ? P[4 * m] : Q[2 * (m - 4)]);
#pragma unroll
    for (int m = 0; m < 8; ++m) KG_ST(24 + m, R[m].r, R[m].i);
    KG_ST(32, e64.r, e64.i);
#pragma unroll
    for (int m = 1; m < 8; ++m) {
      const cf t = cmul(e64, m < 2 ? P[8 * m] : (m < 4 ? Q[4 * (m - 2)] : R[2 * (m - 4)]));
      KG_ST(32 + m, t.r, t.i);
    }
  } else {
    cf cb[2];
    float mx = 0.f;
#pragma unroll
    for (int hh = 0; hh < 2; ++hh) {
      const float br = dt * bf2f(pbr[hh]), bi = dt * bf2f(pbi[hh]);
      const float cr = bf2f(pcr[hh]), ci = bf2f(pci[hh]);
      cb[hh] = cmul({cr, ci}, {br, bi});
      mx = fmaxf(mx, fmaxf(fabsf(cb[hh].r), fabsf(cb[hh].i)));
    }
    const float on = -om;
    const cf e16 = cseed(al, on, 16.f), e32 = cseed(al, on, 32.f), e64 = cseed(al, on, 64.f), e128 = cseed(al, on, 128.f);
    const cf e256 = cseed(al, on, 256.f), e512 = cseed(al, on, 512.f), e1024 = cseed(al, on, 1024.f);
    mx = wave_max(mx);
    int ex = (mx > 0.f && mx < 3.0e38f) ? __builtin_amdgcn_frexp_expf(mx) : 0;
    ex = ex < -100 ? -100 : (ex > 100 ? 100 : ex);
    const float sc = __builtin_amdgcn_ldexpf(1.0f, -ex);
    if (tid == 128) ksc[d] = __builtin_amdgcn_ldexpf(1.0f, ex);
    const cf c0 = (w == 2) ? cb[0] : cb[1];
    const cf cbs = {c0.r * sc, -c0.i * sc};
    cf a0 = cbs, a0b = cmul(cbs, e128), a1 = cmul(cbs, e256), a2 = cmul(cbs, e512), a3 = cmul(cbs, e1024);
#pragma unroll
    for (int i = 0; i < 8; ++i) {
      KG_ST(40 + i, a0.r, a0.i);
      KG_ST(48 + i, a0b.r, a0b.i);
      KG_ST(56 + i, a1.r, a1.i);
      KG_ST(64 + i, a2.r, a2.i);
      KG_ST(72 + i, a3.r, a3.i);
      if (i < 7) { a0 = cmul(a0, e16); a0b = cmul(a0b, e16); a1 = cmul(a1, e32); a2 = cmul(a2, e64); a3 = cmul(a3, e128); }
    }
    k2048 = wave_sum(a3.r * e128.r - a3.i * e128.i);
  }
#undef KG_ST
  if (fr < (NKS + 1) * 32) {
    const u4 f = {(unsigned)wv[0] | ((unsigned)wv[1] << 16), (unsigned)wv[2] | ((unsigned)wv[3] << 16),
                  (unsigned)wv[4] | ((unsigned)wv[5] << 16), (unsigned)wv[6] | ((unsigned)wv[7] << 16)};
    st16(w2f, (NKS + 1) * 32768, (fr * 64 + l) * 16, f);
  }
  STAMP((char*)krev - WS_KREV, 4);
  lbar();
  STAMP((char*)krev - WS_KREV, 5);
  {
    const int g = l >> 4;
    h8 xf[8], yf[8];
#pragma unroll
    for (int k4 = 0; k4 < 4; ++k4) {
      const char* xa = sm + (xbase ^ (k4 << 6));
      const char* ya = sm + (ybase ^ (k4 << 6));
      xf[k4] = *(const h8*)xa; xf[4 + k4] = *(const h8*)(xa + 256);
      yf[k4] = *(const h8*)ya; yf[4 + k4] = *(const h8*)(ya + 256);
    }
    lbar();
    f4 acc0 = {0.f, 0.f, 0.f, 0.f}, acc1 = {0.f, 0.f, 0.f, 0.f};
#pragma unroll
    for (int kk = 0; kk < 4; ++kk) {
      acc0 = __builtin_amdgcn_mfma_f32_16x16x32_f16(xf[2 * kk], yf[2 * kk], acc0, 0, 0, 0);
      acc1 = __builtin_amdgcn_mfma_f32_16x16x32_f16(xf[2 * kk + 1], yf[2 * kk + 1], acc1, 0, 0, 0);
    }
    _Float16* Kc = (_Float16*)sm;
#pragma unroll
    for (int r = 0; r < 4; ++r) {
      const int i = 4 * g + r;
      const bool ok = (i >= ilo) && (i < ihi);
      Kc[ok ? kbase + 16 * i + (l & 15) : 648] = (_Float16)(acc0[r] + acc1[r]);
    }
    if (w >= 2 && l == 0) ((float*)(sm + 1536))[w - 2] = k2048;
  }
  lbar();
  STAMP((char*)krev - WS_KREV, 6);
  {
    const _Float16* Kc = (const _Float16*)sm + ebase;
    h8 o;
#pragma unroll
    for (int e = 0; e < 8; ++e) o[e] = Kc[(epat >> (4 * (7 - e))) & 15];
    if (tid == 0) {
      const float* kp = (const float*)(sm + 1536);
      const _Float16 klast = (_Float16)(kp[0] + kp[1]);
      o[0] = klast; o[1] = klast; o[2] = klast; o[3] = klast;
    }
    st16(krev, SD * KSTR * 2, (d * KSTR + 8 * tid) * 2, __builtin_bit_cast(u4, o));
    if (tid < 8) st16(krev, SD * KSTR * 2, (d * KSTR + 2048 + 8 * tid) * 2, (u4){0u, 0u, 0u, 0u});
  }
}

#define CVP 1
constexpr int CV_KC = 4160;
constexpr int CV_U = 8 * CV_KC;
constexpr int CV_CH = CV_U + 95 * 80;
constexpr int CV_LDS = CV_CH;

DEVI void cv_role(int d, char* sm, const _Float16* __restrict__ xnt, const _Float16* __restrict__ krev,
                  const float* __restrict__ ksc, const u16* __restrict__ dskip, u16* __restrict__ at,
                  unsigned* __restrict__ cnt) {
  __builtin_amdgcn_s_setprio(3);
  const int l = threadIdx.x & 63;
  const int w = __builtin_amdgcn_readfirstlane(threadIdx.x >> 6);
  const int tid = threadIdx.x;
  const u4 uv = *(const u4*)(xnt + (size_t)d * SL + 8 * tid);
  const u4* kg = (const u4*)(krev + (size_t)d * KSTR);
  const u4 k0 = kg[tid], k1 = kg[tid + 1];
  u4 k2 = {0u, 0u, 0u, 0u}, k3 = {0u, 0u, 0u, 0u};
  if (tid < 3) { k2 = kg[256 + tid]; k3 = kg[257 + tid]; }
  const float scale = ksc[d];
  const float dsk = bf2f(dskip[d]);
  if (tid < 155) *(u4*)(sm + CV_U + tid * 16) = (u4){0u, 0u, 0u, 0u};
  *(u4*)(sm + CV_U + (31 + (tid >> 2)) * 80 + (tid & 3) * 16) = uv;
  auto build = [&](u4 a, u4 b, int q) {
    const unsigned wv[8] = {a.x, a.y, a.z, a.w, b.x, b.y, b.z, b.w};
    unsigned f[7];
#pragma unroll
    for (int m = 0; m < 7; ++m) f[m] = __builtin_amdgcn_alignbit(wv[m + 1], wv[m], 16);
#pragma unroll
    for (int m = 0; m < 4; ++m) {
      *(u4*)(sm + (2 * m) * CV_KC + 16 * q) = (u4){wv[m], wv[m + 1], wv[m + 2], wv[m + 3]};
      *(u4*)(sm + (2 * m + 1) * CV_KC + 16 * q) = (u4){f[m], f[m + 1], f[m + 2], f[m + 3]};
    }
  };
  build(k0, k1, tid);
  if (tid < 3) build(k2, k3, 256 + tid);
  lbar();
  STAMP(xnt, 8);
  __builtin_amdgcn_s_setprio(CVP);
  const int ks = w & 1, hb = w >> 1;
  const int b = l & 31, hh = l >> 5;
  const char* abase = sm + (7 - (b & 7)) * CV_KC + 16 * (255 - 4 * (16 * hb + 47) - (b >> 3) + 2 * ks + hh);
  const char* bbase = sm + CV_U + (31 - 16 * hb - 15 + b) * 80 + 32 * ks + 16 * hh;
  f16v t0 = {}, t1 = {};
#pragma unroll
  for (int i = 0; i < 16; ++i) {
    const h8 a_lo = *(const h8*)(abase + 64 * (15 - i) + 2048);
    const h8 a_hi = *(const h8*)(abase + 64 * (15 - i));
    const h8 b_lo = *(const h8*)(bbase + 80 * (15 - i));
    const h8 b_hi = *(const h8*)(bbase + 80 * (15 - i) + 2560);
    t0 = __builtin_amdgcn_mfma_f32_32x32x16_f16(a_lo, b_lo, t0, 0, 0, 0);
    t1 = __builtin_amdgcn_mfma_f32_32x32x16_f16(a_lo, b_hi, t1, 0, 0, 0);
    t1 = __builtin_amdgcn_mfma_f32_32x32x16_f16(a_hi, b_lo, t1, 0, 0, 0);
  }
  __builtin_amdgcn_s_setprio(3);
  lbar();
  STAMP(xnt, 9);
  f4 own[2];
  auto grp = [&](int G, int hf) -> f4 {
    const f16v& t = (G >> 1) ? t1 : t0; const int o = 8 * (G & 1) + 4 * hf;
    return (f4){t[o], t[o + 1], t[o + 2], t[o + 3]};
  };
  sfor<0, 4>([&](auto W) {
    if (w == W.v) {
#pragma unroll
      for (int s = 1; s < 4; ++s)
#pragma unroll
        for (int hf = 0; hf < 2; ++hf)
          *(f4*)(sm + ((((W.v + s) & 3) * 3 + (3 - s)) * 2 + hf) * 1024 + l * 16) = grp((W.v + s) & 3, hf);
      own[0] = grp(W.v, 0); own[1] = grp(W.v, 1);
    }
  });
  const int arow = 32 * (w >> 1) + b;
  const h8 uu = *(const h8*)(sm + CV_U + (31 + arow) * 80 + 32 * (w & 1) + 16 * hh);
  lbar();
#pragma unroll
  for (int sI = 0; sI < 3; ++sI)
#pragma unroll
    for (int hf = 0; hf < 2; ++hf) own[hf] += *(const f4*)(sm + ((w * 3 + sI) * 2 + hf) * 1024 + l * 16);
  {
    unsigned y0 = pk_bf(own[0][0] * scale, own[0][1] * scale), y1 = pk_bf(own[0][2] * scale, own[0][3] * scale);
    unsigned y2 = pk_bf(own[1][0] * scale, own[1][1] * scale), y3 = pk_bf(own[1][2] * scale, own[1][3] * scale);
    const auto ra = __builtin_amdgcn_permlane32_swap(y0, y2, false, false);
    const auto rb = __builtin_amdgcn_permlane32_swap(y1, y3, false, false);
    const unsigned yy[4] = {ra[0], rb[0], ra[1], rb[1]};
    u4 ov;
#pragma unroll
    for (int e = 0; e < 4; ++e) {
      const unsigned v = pk_bf(lo_bf(yy[e]) + bfr((float)uu[2 * e] * dsk), hi_bf(yy[e]) + bfr((float)uu[2 * e + 1] * dsk));
      ov[e] = v == 0xffffffffu ? 0x7fc07fc0u : v;
    }
    *(u4*)(sm + 24576 + arow * 80 + 32 * (w & 1) + 16 * hh) = ov;
  }
  lbar();
  st16(at, SD * SL * 2, (d * SL + 8 * tid) * 2, *(const u4*)(sm + 24576 + (tid >> 2) * 80 + (tid & 3) * 16));
  STAMP(xnt, 10);
  bar();
  if (tid < 8)
    __hip_atomic_fetch_add((GAS unsigned*)(cnt + ((d >> 6) * 8 + tid) * CNT_STR), 1u, __ATOMIC_RELAXED, __HIP_MEMORY_SCOPE_AGENT);
}

constexpr int AUX_SC1_VOL = (int)0x80000010u;
constexpr int G2_LDS = 49152;
#define G2_NB 3

DEVI void g2_role(int bid, char* sm, const u16* __restrict__ x, const u16* __restrict__ hfr,
                  const u16* __restrict__ w2f, u16* at, unsigned* cnt, u16* __restrict__ out) {
  __builtin_amdgcn_s_setprio(1);
  const int tid = threadIdx.x, l = tid & 63;
  const int w = __builtin_amdgcn_readfirstlane(tid >> 6);
  const int xcd = bid & 7, idx = bid >> 3;
  const int pm = (xcd >> 1) * 8 + (idx >> 2), pn = (xcd & 1) * 4 + (idx & 3);
  const int t0 = pm * 64, d0 = pn * 64;
  const int g = l >> 4, li = l & 15, q = li >> 2, p = l & 3;
  const int k0 = 11 * w;

  const __amdgpu_buffer_rsrc_t rh = __builtin_amdgcn_make_buffer_rsrc((void*)hfr, 0, 128 * NKP * 1024, 0x00020000);
  const __amdgpu_buffer_rsrc_t rw = __builtin_amdgcn_make_buffer_rsrc((void*)w2f, 0, NKP * 32768, 0x00020000);
  const int lo = l * 16;
  int hso[4];
#pragma unroll
  for (int s = 0; s < 4; ++s) hso[s] = ((pm * 4 + ((w + s) & 3)) * NKP + k0) * 1024;
  const int wso = (k0 * 32 + pn * 4) * 1024;
  u4 hf[G2_NB][4], wf[G2_NB][4];
  const int rot = (((idx >> 2) + 3 * (idx & 3)) * 23) >> 4;
  auto stp = [&](int i) { int v = i + rot; v = v >= 22 ? v - 22 : v; return v >= 11 ? v - 11 : v; };
  auto ldw = [&](int buf, int i) {
    const int so = wso + stp(i) * 32768;
#pragma unroll
    for (int j = 0; j < 4; ++j) wf[buf][j] = __builtin_amdgcn_raw_buffer_load_b128(rw, lo + j * 1024, so, 0);
  };
  auto ldh = [&](int buf, int i, int s) { hf[buf][s] = __builtin_amdgcn_raw_buffer_load_b128(rh, lo, hso[s] + stp(i) * 1024, 0); };
  f4 acc[4][4];
#pragma unroll
  for (int j = 0; j < 4; ++j)
#pragma unroll
    for (int s = 0; s < 4; ++s) acc[j][s] = (f4){0.f, 0.f, 0.f, 0.f};
#pragma unroll
  for (int i = 0; i < G2_NB; ++i) {
    ldw(i, i);
#pragma unroll
    for (int s = 0; s < 4; ++s) ldh(i, i, s);
    __builtin_amdgcn_sched_barrier(0);
  }
  u2 xr[4];
  const __amdgpu_buffer_rsrc_t rs = __builtin_amdgcn_make_buffer_rsrc((void*)at, 0, SD * SL * 2, 0x00020000);
  sfor<0, 11>([&](auto I) {
    constexpr int i = I.v, b = i % G2_NB;
#pragma unroll
    for (int s = 0; s < 4; ++s) {
#pragma unroll
      for (int j = 0; j < 4; ++j)
        acc[j][s] = __builtin_amdgcn_mfma_f32_16x16x32_bf16(__builtin_bit_cast(b8, wf[b][j]), __builtin_bit_cast(b8, hf[b][s]), acc[j][s], 0, 0, 0);
      __builtin_amdgcn_sched_barrier(0);
      if constexpr (i + G2_NB < 11) { ldh(b, i + G2_NB, s); __builtin_amdgcn_sched_barrier(0); }
    }
    if constexpr (i + G2_NB < 11) { ldw(b, i + G2_NB); __builtin_amdgcn_sched_barrier(0); }
    if constexpr (i == 8) {
#pragma unroll
      for (int j = 0; j < 4; ++j) xr[j] = *(const u2*)(x + (size_t)(t0 + 16 * w + li) * SD + d0 + 16 * j + 4 * g);
      __builtin_amdgcn_sched_barrier(0);
    }
  });
  const int aoff = ((d0 + l) * SL + t0 + 16 * w) * 2;
  u4 av0 = __builtin_amdgcn_raw_buffer_load_b128(rs, aoff, 0, AUX_SC1_VOL);
  u4 av1 = __builtin_amdgcn_raw_buffer_load_b128(rs, aoff + 16, 0, AUX_SC1_VOL);
#pragma unroll
  for (int s = 1; s < 4; ++s)
#pragma unroll
    for (int j = 0; j < 4; ++j) *(f4*)(sm + ((((w + s) & 3) * 3 + (3 - s)) * 4 + j) * 1024 + l * 16) = acc[j][s];
  STAMP((char*)hfr - WS_H, 8);
  lbar();
  f4 v[4];
#pragma unroll
  for (int j = 0; j < 4; ++j) {
    v[j] = acc[j][0];
#pragma unroll
    for (int sI = 0; sI < 3; ++sI) v[j] += *(const f4*)(sm + ((w * 3 + sI) * 4 + j) * 1024 + l * 16);
    PIN(v[j]);
  }
  for (unsigned spins = 0;; ++spins) {
    const bool miss = (av0.x == 0xffffffffu) | (av0.y == 0xffffffffu) | (av0.z == 0xffffffffu) | (av0.w == 0xffffffffu) |
                      (av1.x == 0xffffffffu) | (av1.y == 0xffffffffu) | (av1.z == 0xffffffffu) | (av1.w == 0xffffffffu);
    if (!__any(miss) || spins > (1u << 16)) break;
    if (spins == 0) {
      GAS unsigned* c = (GAS unsigned*)(cnt + (pn * 8 + (pm & 7)) * CNT_STR);
      for (unsigned sp = 0; sp < (1u << 18); ++sp) {
        const unsigned cv = __builtin_amdgcn_readfirstlane(__hip_atomic_load(c, __ATOMIC_RELAXED, __HIP_MEMORY_SCOPE_AGENT));
        if (cv >= 64u) break;
        __builtin_amdgcn_s_sleep(8);
      }
    } else {
      __builtin_amdgcn_s_sleep(2);
    }
    if (miss) {
      av0 = __builtin_amdgcn_raw_buffer_load_b128(rs, aoff, 0, AUX_SC1_VOL);
      av1 = __builtin_amdgcn_raw_buffer_load_b128(rs, aoff + 16, 0, AUX_SC1_VOL);
    }
  }
  STAMP((char*)hfr - WS_H, 9);
  asm volatile("s_waitcnt lgkmcnt(0)" ::: "memory");
  char* myR = sm + w * 12288;
  *(u4*)(myR + l * 32) = av0;
  *(u4*)(myR + l * 32 + 16) = av1;
  asm volatile("s_waitcnt lgkmcnt(0)" ::: "memory");
  STAMP((char*)hfr - WS_H, 10);
  u2 ov[4];
#pragma unroll
  for (int j = 0; j < 4; ++j) {
    const b4 a4 = trread(myR + (16 * j + 4 * g + q) * 32 + 8 * p);
    const u2 xv = xr[j];
    const float xs[4] = {lo_bf(xv.x), hi_bf(xv.x), lo_bf(xv.y), hi_bf(xv.y)};
    float o[4];
#pragma unroll
    for (int r = 0; r < 4; ++r) o[r] = bfr(xs[r] + (float)a4[r]) + bfr(v[j][r]);
    ov[j] = (u2){pk_bf(o[0], o[1]), pk_bf(o[2], o[3])};
  }
  const unsigned oo = ((t0 + 16 * w + li) * SD + d0 + 16 * (g & 1) + 8 * (g >> 1)) * 2;
  st16(out, SL * SD * 2, oo, pair16(ov[0], ov[1]));
  st16(out, SL * SD * 2, oo + 64, pair16(ov[2], ov[3]));
}

constexpr int P1_LDS = G1_LDS > KG_LDS ? G1_LDS : KG_LDS;
constexpr int P2_LDS = G2_LDS > CV_LDS ? G2_LDS : CV_LDS;

__global__ __launch_bounds__(512, 4) void k_phase1(const u16* x, const u16* Are, const u16* Aim, const u16* Bre,
                                                   const u16* Bim, const u16* Cre, const u16* Cim, const u16* logdt,
                                                   const u16* w1, const u16* w2, char* ws, int boff) {
  extern __shared__ __attribute__((aligned(16))) char sm[];
  const int bid = blockIdx.x + boff;
  STAMP(ws, 0);
  if (bid >= 256) {
    if (bid == 256 && threadIdx.x < 64) ((unsigned*)(ws + WS_CNT))[threadIdx.x * CNT_STR] = 0u;
    g1_role(bid - 256, sm, x, w1, (u16*)(ws + WS_H), (_Float16*)(ws + WS_XNT));
  } else {
    kg_role(bid, sm, Are, Aim, Bre, Bim, Cre, Cim, logdt, w2, (_Float16*)(ws + WS_KREV), (float*)(ws + WS_KSC),
            (u16*)(ws + WS_W2F), (u16*)(ws + WS_AT));
  }
  STAMP(ws, 1);
}

__global__ __launch_bounds__(256, 3) void k_phase2(const u16* x, const u16* dskip, char* ws, u16* out, int boff) {
  extern __shared__ __attribute__((aligned(16))) char sm[];
  const int bid = blockIdx.x + boff;
  STAMP(ws, 2);
  if (bid >= 512) {
    g2_role(bid - 512, sm, x, (const u16*)(ws + WS_H), (const u16*)(ws + WS_W2F), (u16*)(ws + WS_AT), (unsigned*)(ws + WS_CNT), out);
  } else {
    cv_role(bid, sm, (const _Float16*)(ws + WS_XNT), (const _Float16*)(ws + WS_KREV), (const float*)(ws + WS_KSC),
            dskip, (u16*)(ws + WS_AT), (unsigned*)(ws + WS_CNT));
  }
  STAMP(ws, 3);
}

static void launch_p1(void* const* d_in, void* d_ws, hipStream_t stream, int grid, int boff) {
  (void)hipFuncSetAttribute((const void*)k_phase1, hipFuncAttributeMaxDynamicSharedMemorySize, P1_LDS);
  k_phase1<<<grid, 512, P1_LDS, stream>>>((const u16*)d_in[0], (const u16*)d_in[1], (const u16*)d_in[2], (const u16*)d_in[3],
                                          (const u16*)d_in[4], (const u16*)d_in[5], (const u16*)d_in[6],
                                          (const u16*)d_in[7], (const u16*)d_in[10], (const u16*)d_in[11], (char*)d_ws, boff);
}
static void launch_p2(void* const* d_in, void* d_out, void* d_ws, hipStream_t stream, int grid, int boff) {
  (void)hipFuncSetAttribute((const void*)k_phase2, hipFuncAttributeMaxDynamicSharedMemorySize, P2_LDS);
  k_phase2<<<grid, 256, P2_LDS, stream>>>((const u16*)d_in[0], (const u16*)d_in[8], (char*)d_ws, (u16*)d_out, boff);
}

extern "C" void kernel_launch(void* const* d_in, const int* in_sizes, int n_in, void* d_out, int out_size,
                              void* d_ws, size_t ws_size, hipStream_t stream) {
  launch_p1(d_in, d_ws, stream, 512, 0);
  launch_p2(d_in, d_out, d_ws, stream, 768, 0);
}
```

```cpp
#include <hip/hip_runtime.h>
#include <hip/hip_bf16.h>
#include <stdint.h>

typedef unsigned short u16;
typedef _Float16 h8 __attribute__((ext_vector_type(8)));
typedef __bf16 b8 __attribute__((ext_vector_type(8)));
typedef __bf16 b4 __attribute__((ext_vector_type(4)));
typedef __bf16 b2 __attribute__((ext_vector_type(2)));
typedef float f4 __attribute__((ext_vector_type(4)));
typedef float f16v __attribute__((ext_vector_type(16)));
typedef unsigned u4 __attribute__((ext_vector_type(4)));
typedef unsigned u2 __attribute__((ext_vector_type(2)));
#define LAS __attribute__((address_space(3)))
#define GAS __attribute__((address_space(1)))
#define DEVI __device__ __forceinline__

constexpr int SL = 2048, SD = 512, SN = 128, SF = 1376;
constexpr int FL_A = 1, FL_A2 = 9, FL_W = 41, FL_C = 45;
constexpr int NKS = SF / 32;
constexpr int NKP = NKS + 1;
constexpr size_t WS_XNT = 0;
constexpr size_t WS_FLG = WS_XNT + (size_t)SD * SL * 2;
constexpr size_t WS_H = WS_FLG + 32768;
constexpr size_t WS_AT = WS_H + (size_t)128 * NKP * 1024;
constexpr size_t WS_W2F = WS_AT + (size_t)SD * SL * 2;

#define STAMP(ws, slot) do {} while (0)
DEVI float bf2f(u16 v) { return __uint_as_float(((unsigned)v) << 16); }
DEVI float bfr(float f) { return (float)(__bf16)f; }
DEVI unsigned pk_bf(float a, float b) { b2 v = {(__bf16)a, (__bf16)b}; return __builtin_bit_cast(unsigned, v); }
DEVI unsigned pk_h(float a, float b) { unsigned r; asm("v_cvt_pk_f16_f32 %0, %1, %2" : "=v"(r) : "v"(a), "v"(b)); return r; }
DEVI float lo_bf(unsigned v) { return __uint_as_float(v << 16); }
DEVI float hi_bf(unsigned v) { return __uint_as_float(v & 0xffff0000u); }
template <int N> DEVI void wait_vm() { asm volatile("s_waitcnt vmcnt(%0) lgkmcnt(0)" ::"n"(N) : "memory"); }
DEVI void bar() { asm volatile("" ::: "memory"); __builtin_amdgcn_s_barrier(); asm volatile("" ::: "memory"); }
DEVI void lbar() { asm volatile("s_waitcnt lgkmcnt(0)" ::: "memory"); __builtin_amdgcn_s_barrier(); asm volatile("" ::: "memory"); }
DEVI void glds16(const void* g, char* l) {
  __builtin_amdgcn_global_load_lds((const GAS void*)g, (LAS void*)l, 16, 0, 0);
}
DEVI b4 trread(const char* p) { return __builtin_amdgcn_ds_read_tr16_b64_v4bf16((LAS b4*)p); }
DEVI b8 cat(b4 a, b4 b) { return __builtin_shufflevector(a, b, 0, 1, 2, 3, 4, 5, 6, 7); }
template <int V> struct IC { static constexpr int v = V; };
template <int I, int N, class F> DEVI void sfor(F&& f) { if constexpr (I < N) { f(IC<I>{}); sfor<I + 1, N>(f); } }
DEVI void st16(void* base, unsigned bytes, unsigned off, u4 v) {
  __builtin_amdgcn_raw_buffer_store_b128(v, __builtin_amdgcn_make_buffer_rsrc(base, 0, bytes, 0x00020000), off, 0, 16);
}
DEVI u4 pair16(u2 a, u2 b) {
  const auto rx = __builtin_amdgcn_permlane16_swap(a.x, b.x, false, false);
  const auto ry = __builtin_amdgcn_permlane16_swap(a.y, b.y, false, false);
  return (u4){rx[0], ry[0], rx[1], ry[1]};
}
template <int CTRL, int RM> DEVI float dppf(float v) {
  return __builtin_bit_cast(float, __builtin_amdgcn_update_dpp(0, __builtin_bit_cast(int, v), CTRL, RM, 0xf, false));
}
DEVI float wave_max(float v) {
  v = fmaxf(v, dppf<0x111, 0xf>(v)); v = fmaxf(v, dppf<0x112, 0xf>(v)); v = fmaxf(v, dppf<0x114, 0xf>(v));
  v = fmaxf(v, dppf<0x118, 0xf>(v)); v = fmaxf(v, dppf<0x142, 0xa>(v)); v = fmaxf(v, dppf<0x143, 0xc>(v));
  return __builtin_bit_cast(float, __builtin_amdgcn_readlane(__builtin_bit_cast(int, v), 63));
}
DEVI float wave_sum(float v) {
  v += dppf<0x111, 0xf>(v); v += dppf<0x112, 0xf>(v); v += dppf<0x114, 0xf>(v);
  v += dppf<0x118, 0xf>(v); v += dppf<0x142, 0xa>(v); v += dppf<0x143, 0xc>(v);
  return __builtin_bit_cast(float, __builtin_amdgcn_readlane(__builtin_bit_cast(int, v), 63));
}
DEVI void flag_set(unsigned* flg, int line, int word, unsigned tag) {
  __hip_atomic_store((GAS unsigned*)(flg + line * 64 + word), tag, __ATOMIC_RELAXED, __HIP_MEMORY_SCOPE_AGENT);
}
DEVI void flag_wait(unsigned* flg, int off, bool act, unsigned tag) {
  for (unsigned sp = 0; sp < (1u << 18); ++sp) {
    unsigned v = tag;
    if (act) v = __hip_atomic_load((GAS unsigned*)(flg + off), __ATOMIC_RELAXED, __HIP_MEMORY_SCOPE_AGENT);
    if (__all(v == tag)) break;
    __builtin_amdgcn_s_sleep(6);
  }
  asm volatile("" ::: "memory");
}
constexpr int AUX_SC1_VOL = (int)0x80000010u;
#define PIN(x) asm volatile("" : "+v"(x))
__device__ const unsigned g_ones[4] = {0x3f803f80u, 0x3f803f80u, 0x3f803f80u, 0x3f803f80u};

constexpr int G1_S = 5;
constexpr int G1_STAGE = 16384;
constexpr int G1_LDS = G1_S * G1_STAGE;

template <int WM>
DEVI void g1_body(int bid, char* sm, const u16* __restrict__ x, const u16* __restrict__ w1,
                  u16* __restrict__ h, _Float16* __restrict__ xnt, int w) {
  const int tid = threadIdx.x, l = tid & 63;
  const int wn = w & 3;
  const int xcd = bid & 7, idx = bid >> 3;
  const int pm = (xcd >> 1) * 8 + (idx >> 2), pn = (xcd & 1) * 4 + (idx & 3);
  const int r0 = pm * 64, c0 = pn * 176;
  const int g = l >> 4, li = l & 15, q = li >> 2, p = l & 3;

  const char* ga[2];
  unsigned gs[2];
#pragma unroll
  for (int j = 0; j < 2; ++j) {
    const int pc = 2 * w + j;
    if (pc < 4) {
      const int xrow = 16 * pc + (l >> 2);
      ga[j] = (const char*)(x + (size_t)(r0 + xrow) * SD + (((l & 3) ^ ((xrow >> 2) & 3)) * 8));
      gs[j] = 64;
    } else {
      const int ci = (pc - 4) * 64 + l, k = ci / 24, cp = ci - k * 24;
      const int c = cp ^ (((k >> 1) & 3) << 1);
      int col = c0 + 8 * c; if (col > SF - 8) col = SF - 8;
      ga[j] = c >= 22 ? (const char*)g_ones : (const char*)(w1 + (size_t)k * SF + col);
      gs[j] = c >= 22 ? 0 : 32 * SF * 2;
    }
  }
  char* const lb = sm + w * 2048;
  unsigned kc = (2 * (idx >> 2) + 4 * (idx & 3)) & 15;
  auto issue = [&](int slot, int j) {
    glds16(ga[j] + (unsigned long)kc * gs[j], lb + slot * G1_STAGE + j * 1024);
    if (j == 1) kc = (kc + 1) & 15;
  };
#pragma unroll
  for (int s = 0; s < G1_S; ++s) { issue(s, 0); issue(s, 1); }

  const int mt0 = 2 * WM + (wn & 1), mt1 = 2 * WM + ((wn & 1) ^ 1);
  const int xo0 = li * 64 + (((g >> 1) ^ q) * 16) + 8 * (g & 1), xo1 = li * 64 + (((2 + (g >> 1)) ^ q) * 16) + 8 * (g & 1);
  const char* xa00 = sm + mt0 * 1024 + xo0; const char* xa01 = sm + mt0 * 1024 + xo1;
  const char* xa10 = sm + mt1 * 1024 + xo0; const char* xa11 = sm + mt1 * 1024 + xo1;
  const char* wa[3];
#pragma unroll
  for (int j = 0; j < 3; ++j) {
    const int row = 4 * g + q;
    wa[j] = sm + 4096 + row * 384 + (((3 * wn + j) ^ ((row >> 1) & 3)) * 32) + 8 * p;
  }
  const int pi = tid >> 4, c4 = tid & 15;
  unsigned hoff[3];
#pragma unroll
  for (int j = 0; j < 3; ++j) {
    const int col = c0 + 16 * (3 * wn + j) + 8 * (g >> 1);
    const bool ok = (3 * wn + j < 11);
    hoff[j] = ok ? (unsigned)((((pm * 4 + ((g & 1) ? mt1 : mt0)) * NKP + (col >> 5)) * 64 + ((col >> 3) & 3) * 16 + li) * 16) : 0x80000000u;
    PIN(hoff[j]);
  }
  u2 xva, xvb;

  f4 acc[2][3], cs[3], rq = {0.f, 0.f, 0.f, 0.f};
#pragma unroll
  for (int j = 0; j < 3; ++j) { acc[0][j] = acc[1][j] = cs[j] = (f4){0.f, 0.f, 0.f, 0.f}; }
  const b8 ones = {(__bf16)1.0f, (__bf16)1.0f, (__bf16)1.0f, (__bf16)1.0f, (__bf16)1.0f, (__bf16)1.0f, (__bf16)1.0f, (__bf16)1.0f};
  b8 xf[2][2], wf[2][3];
  auto rd = [&](int buf, int so) {
#pragma unroll
    for (int j = 0; j < 3; ++j) wf[buf][j] = cat(trread(wa[j] + so), trread(wa[j] + so + 16 * 384));
    xf[buf][0] = cat(*(const b4*)(xa00 + so), *(const b4*)(xa01 + so));
    xf[buf][1] = cat(*(const b4*)(xa10 + so), *(const b4*)(xa11 + so));
  };
  wait_vm<2 * (G1_S - 1)>();
  bar();
  rd(0, 0);
  sfor<0, 16>([&](auto K) {
    constexpr int k = K.v, u = k & 1;
    if constexpr (k < 15) {
      constexpr int later = ((k + G1_S) < 16 ? (k + G1_S) : 16) - (k + 2);
      wait_vm<2 * later + (k > 11 ? 2 : 0)>();
      bar();
      if constexpr (k + G1_S < 16) issue(k % G1_S, 0);
      if constexpr (k == 11) {
        xva = *(const u2*)(x + (size_t)(r0 + 2 * pi) * SD + 64 * pn + 4 * c4);
        xvb = *(const u2*)(x + (size_t)(r0 + 2 * pi + 1) * SD + 64 * pn + 4 * c4);
      }
      rd(u ^ 1, ((k + 1) % G1_S) * G1_STAGE);
    }
    __builtin_amdgcn_sched_barrier(0);
#pragma unroll
    for (int j = 0; j < 3; ++j) acc[0][j] = __builtin_amdgcn_mfma_f32_16x16x32_bf16(wf[u][j], xf[u][0], acc[0][j], 0, 0, 0);
    rq = __builtin_amdgcn_mfma_f32_16x16x32_bf16(xf[u][0], xf[u][0], rq, 0, 0, 0);
    __builtin_amdgcn_sched_barrier(0);
    if constexpr (k + G1_S < 16) issue(k % G1_S, 1);
    __builtin_amdgcn_sched_barrier(0);
#pragma unroll
    for (int j = 0; j < 3; ++j) acc[1][j] = __builtin_amdgcn_mfma_f32_16x16x32_bf16(wf[u][j], xf[u][1], acc[1][j], 0, 0, 0);
    if constexpr (u == WM) {
#pragma unroll
      for (int j = 0; j < 3; ++j) cs[j] = __builtin_amdgcn_mfma_f32_16x16x32_bf16(wf[u][j], ones, cs[j], 0, 0, 0);
    }
    __builtin_amdgcn_sched_barrier(0);
  });
  STAMP(xnt, 4);
  PIN(xva); PIN(xvb);
  float* raw = (float*)(sm + 2 * G1_STAGE);
  float2* stats = (float2*)(sm + 2 * G1_STAGE + 1024);
  float* csum = (float*)(sm + 2 * G1_STAGE + 2048);
  if (wn == 3 && g == 0) { raw[16 * mt0 + li] = acc[0][2][0]; raw[16 * mt1 + li] = acc[1][2][0]; }
  if (wn < 2 && g == q) raw[64 + 16 * mt0 + li] = p == 0 ? rq[0] : (p == 1 ? rq[1] : (p == 2 ? rq[2] : rq[3]));
  if (li == 0) {
#pragma unroll
    for (int j = 0; j < 3; ++j) *(f4*)(csum + WM * 192 + 16 * (3 * wn + j) + 4 * g) = cs[j];
  }
  lbar();
  if (tid < 64) {
    const float mean = raw[tid] * (1.0f / SD);
    float var = raw[64 + tid] * (1.0f / SD) - mean * mean; var = var < 0.f ? 0.f : var;
    stats[tid] = make_float2(bfr(mean), bfr(__builtin_amdgcn_sqrtf(bfr(bfr(var) + 1e-5f))));
  }
  f4 csr[3];
#pragma unroll
  for (int j = 0; j < 3; ++j)
    csr[j] = *(const f4*)(csum + 16 * (3 * wn + j) + 4 * g) + *(const f4*)(csum + 192 + 16 * (3 * wn + j) + 4 * g);
  lbar();
  unsigned* T = (unsigned*)(sm + 3 * G1_STAGE);
  {
    const float2 s0 = stats[2 * pi], s1 = stats[2 * pi + 1];
    const float i0 = __builtin_amdgcn_rcpf(s0.y), i1 = __builtin_amdgcn_rcpf(s1.y);
#pragma unroll
    for (int e = 0; e < 4; ++e) {
      const unsigned wva = xva[e >> 1], wvb = xvb[e >> 1];
      const float a = (e & 1) ? hi_bf(wva) : lo_bf(wva), b = (e & 1) ? hi_bf(wvb) : lo_bf(wvb);
      const float na = bfr(bfr(a - s0.x) * i0), nb = bfr(bfr(b - s1.x) * i1);
      T[(4 * c4 + e) * 32 + (pi ^ ((c4 >> 1) << 2))] = pk_h(na, nb);
    }
  }
  {
    const float2 st0 = stats[16 * mt0 + li], st1 = stats[16 * mt1 + li];
    const float inv0 = __builtin_amdgcn_rcpf(st0.y), inv1 = __builtin_amdgcn_rcpf(st1.y);
#pragma unroll
    for (int j = 0; j < 3; ++j) {
      float v[4], u[4];
#pragma unroll
      for (int r = 0; r < 4; ++r) {
        v[r] = fmaxf((acc[0][j][r] - st0.x * csr[j][r]) * inv0, 0.f);
        u[r] = fmaxf((acc[1][j][r] - st1.x * csr[j][r]) * inv1, 0.f);
      }
      u4 o = pair16((u2){pk_bf(v[0], v[1]), pk_bf(v[2], v[3])}, (u2){pk_bf(u[0], u[1]), pk_bf(u[2], u[3])});
      if (c0 + 16 * (3 * wn + j) >= SF) o = (u4){0u, 0u, 0u, 0u};
      st16(h, 128 * NKP * 1024, hoff[j], o);
    }
  }
  {
    lbar();
    const int ch = tid >> 3, m = tid & 7;
    const u4 v = *(const u4*)(T + ch * 32 + 4 * (m ^ (ch >> 3)));
    st16(xnt, SD * SL * 2, ((64 * pn + ch) * SL + r0 + 8 * m) * 2, v);
  }
}

DEVI void g1_role(int bid, char* sm, const u16* __restrict__ x, const u16* __restrict__ w1,
                  u16* __restrict__ h, _Float16* __restrict__ xnt, unsigned* flg, unsigned tag) {
  const int w = __builtin_amdgcn_readfirstlane(threadIdx.x >> 6);
  __builtin_amdgcn_s_setprio(2);
  if (w < 4) g1_body<0>(bid, sm, x, w1, h, xnt, w); else g1_body<1>(bid, sm, x, w1, h, xnt, w);
  asm volatile("s_waitcnt vmcnt(0)" ::: "memory");
  lbar();
  if (threadIdx.x < 2) {
    const int xcd = bid & 7, idx = bid >> 3;
    const int pm = (xcd >> 1) * 8 + (idx >> 2), pn = (xcd & 1) * 4 + (idx & 3);
    if (threadIdx.x == 0) flag_set(flg, FL_A + pn, pm, tag); else flag_set(flg, FL_A2 + pm, pn, tag);
  }
}

#define CVP 1
constexpr int KG_CH = 80 * 512;
constexpr int KC_LDS = 2 * KG_CH;
constexpr int CV_KC = 4160;
constexpr int CV_U = 8 * CV_KC;

struct cf { float r, i; };
DEVI cf cmul(cf a, cf b) {
  float t, u, re, im;
  asm("v_mul_f32 %0, %2, %3\n\tv_mul_f32 %1, %2, %4" : "=&v"(t), "=&v"(u) : "v"(a.i), "v"(b.i), "v"(b.r));
  asm("v_fma_f32 %0, %2, %3, -%4\n\tv_fma_f32 %1, %2, %5, %6" : "=&v"(re), "=&v"(im) : "v"(a.r), "v"(b.r), "v"(t), "v"(b.i), "v"(u));
  return {re, im};
}
DEVI cf cseed(float al, float om, float m) {
  const float mag = __builtin_amdgcn_exp2f((al * m) * 1.44269504088896341f);
  const float ph = om * m;
  const float chi = 0.15915494f, clo = 6.4206382e-9f;
  const float fr = __builtin_fmaf(ph, clo, __builtin_fmaf(ph, chi, -__builtin_rintf(ph * chi)));
  return {mag * __builtin_amdgcn_cosf(fr), mag * __builtin_amdgcn_sinf(fr)};
}

DEVI void kc_role(int kb, char* sm0, const u16* __restrict__ Are, const u16* __restrict__ Aim,
                  const u16* __restrict__ Bre, const u16* __restrict__ Bim, const u16* __restrict__ Cre,
                  const u16* __restrict__ Cim, const u16* __restrict__ logdt, const u16* __restrict__ w2,
                  const u16* __restrict__ dskip, _Float16* xnt, u16* __restrict__ w2f, u16* __restrict__ at,
                  unsigned* flg, unsigned tag) {
  const int l = threadIdx.x & 63;
  const int w8 = __builtin_amdgcn_readfirstlane(threadIdx.x >> 6);
  const int w = w8 & 3, tid = threadIdx.x & 255;
  const int d = 2 * kb + (w8 >> 2);
  char* sm = sm0 + (w8 >> 2) * KG_CH;
  const int n = tid & 127;
  __builtin_amdgcn_s_setprio(3);
  const u16 ldt = logdt[d], are = Are[d * SN + n], aim = Aim[d * SN + n], dsk16 = dskip[d];
  u16 pbr[2], pbi[2], pcr[2], pci[2];
#pragma unroll
  for (int hh = 0; hh < 2; ++hh) {
    const int nn = d * SN + (n & 63) + 64 * hh;
    pbr[hh] = Bre[nn]; pbi[hh] = Bim[nn]; pcr[hh] = Cre[nn]; pci[hh] = Cim[nn];
  }
  __builtin_amdgcn_sched_barrier(0);
  const int fr = kb * 8 + w8;
  u16 wv[8] = {0, 0, 0, 0, 0, 0, 0, 0};
  if (fr < NKS * 32) {
    const u16* src = w2 + (size_t)(32 * (fr >> 5) + 8 * (l >> 4)) * SD + 16 * (fr & 31) + (l & 15);
#pragma unroll
    for (int e = 0; e < 8; ++e) wv[e] = src[(size_t)e * SD];
  }
  __builtin_amdgcn_s_setprio(0);
  int xr, yr;
  {
    const int li = l & 15, y = li << w;
    yr = y < 16 ? y : (y < 32 ? 8 + (y >> 1) : (y < 64 ? 16 + (y >> 2) : 24 + (y >> 3)));
    xr = 40 + (w == 0 ? li : (w == 3 ? 32 + (li & 7) : 16 + li));
  }
  const int xbase = xr * 512 + (((l >> 4) ^ (xr & 15)) << 4), ybase = yr * 512 + (((l >> 4) ^ (yr & 15)) << 4);
  int ebase[2]; unsigned epat[2];
#pragma unroll
  for (int j = 0; j < 2; ++j) {
    const int m = 255 - tid - j;
    if (m < 0) { ebase[j] = 656; epat[j] = 0u; }
    else if (m < 32) { ebase[j] = 8 * m; epat[j] = 0x76543210u; }
    else if (m < 64) { ebase[j] = 128 + 4 * m; epat[j] = 0x43222100u; }
    else if (m < 128) { ebase[j] = 256 + 2 * m; epat[j] = 0x22111000u; }
    else { ebase[j] = 384 + m; epat[j] = (m & 1) ? 0x11110000u : 0x11100000u; }
  }
  const int kbase = w == 0 ? 0 : (w == 3 ? 512 : 256), ilo = w == 2 ? 8 : 0, ihi = (w & 1) ? 8 : 16;
  const float dt = bfr(expf(bf2f(ldt)));
  const float al = dt * bf2f(are), om = dt * bf2f(aim);
  const int wa = (n >> 2) * 16 + (n & 3) * 4;
#define KG_ST(row, re, im) *(unsigned*)(sm + (row) * 512 + (wa ^ (((row) & 15) << 4))) = pk_h(re, im)
  float k2048 = 0.f, kscale = 1.f;
  if (w < 2) {
    cf P[16], Q[8], R[8];
    const cf e1 = cseed(al, om, 1.f), e16 = cseed(al, om, 16.f), e32 = cseed(al, om, 32.f), e64 = cseed(al, om, 64.f);
    P[0] = {1.f, 0.f}; P[1] = e1;
#pragma unroll
    for (int j = 2; j < 16; ++j) P[j] = cmul(P[j >> 1], P[j - (j >> 1)]);
#pragma unroll
    for (int j = 0; j < 16; ++j) KG_ST(j, P[j].r, P[j].i);
    Q[0] = e16;
#pragma unroll
    for (int m = 1; m < 8; ++m) Q[m] = cmul(e16, P[2 * m]);
#pragma unroll
    for (int m = 0; m < 8; ++m) KG_ST(16 + m, Q[m].r, Q[m].i);
    R[0] = e32;
#pragma unroll
    for (int m = 1; m < 8; ++m) R[m] = cmul(e32, m < 4 ? P[4 * m] : Q[2 * (m - 4)]);
#pragma unroll
    for (int m = 0; m < 8; ++m) KG_ST(24 + m, R[m].r, R[m].i);
    KG_ST(32, e64.r, e64.i);
#pragma unroll
    for (int m = 1; m < 8; ++m) {
      const cf t = cmul(e64, m < 2 ? P[8 * m] : (m < 4 ? Q[4 * (m - 2)] : R[2 * (m - 4)]));
      KG_ST(32 + m, t.r, t.i);
    }
  } else {
    cf cb[2];
    float mx = 0.f;
#pragma unroll
    for (int hh = 0; hh < 2; ++hh) {
      const float br = dt * bf2f(pbr[hh]), bi = dt * bf2f(pbi[hh]);
      const float cr = bf2f(pcr[hh]), ci = bf2f(pci[hh]);
      cb[hh] = cmul({cr, ci}, {br, bi});
      mx = fmaxf(mx, fmaxf(fabsf(cb[hh].r), fabsf(cb[hh].i)));
    }
    const float on = -om;
    const cf e16 = cseed(al, on, 16.f), e32 = cseed(al, on, 32.f), e64 = cseed(al, on, 64.f), e128 = cseed(al, on, 128.f);
    const cf e256 = cseed(al, on, 256.f), e512 = cseed(al, on, 512.f), e1024 = cseed(al, on, 1024.f);
    mx = wave_max(mx);
    int ex = (mx > 0.f && mx < 3.0e38f) ? __builtin_amdgcn_frexp_expf(mx) : 0;
    ex = ex < -100 ? -100 : (ex > 100 ? 100 : ex);
    const float sc = __builtin_amdgcn_ldexpf(1.0f, -ex);
    kscale = __builtin_amdgcn_ldexpf(1.0f, ex);
    const cf c0 = (w == 2) ? cb[0] : cb[1];
    const cf cbs = {c0.r * sc, -c0.i * sc};
    cf a0 = cbs, a0b = cmul(cbs, e128), a1 = cmul(cbs, e256), a2 = cmul(cbs, e512), a3 = cmul(cbs, e1024);
#pragma unroll
    for (int i = 0; i < 8; ++i) {
      KG_ST(40 + i, a0.r, a0.i);
      KG_ST(48 + i, a0b.r, a0b.i);
      KG_ST(56 + i, a1.r, a1.i);
      KG_ST(64 + i, a2.r, a2.i);
      KG_ST(72 + i, a3.r, a3.i);
      if (i < 7) { a0 = cmul(a0, e16); a0b = cmul(a0b, e16); a1 = cmul(a1, e32); a2 = cmul(a2, e64); a3 = cmul(a3, e128); }
    }
    k2048 = wave_sum(a3.r * e128.r - a3.i * e128.i);
  }
#undef KG_ST
  if (fr < (NKS + 1) * 32) {
    const u4 f = {(unsigned)wv[0] | ((unsigned)wv[1] << 16), (unsigned)wv[2] | ((unsigned)wv[3] << 16),
                  (unsigned)wv[4] | ((unsigned)wv[5] << 16), (unsigned)wv[6] | ((unsigned)wv[7] << 16)};
    st16(w2f, (NKS + 1) * 32768, (fr * 64 + l) * 16, f);
  }
  STAMP(xnt, 4);
  asm volatile("s_waitcnt vmcnt(0)" ::: "memory");
  lbar();
  if (threadIdx.x == 0 && kb < NKP * 4) flag_set(flg, FL_W + (kb & 3), kb >> 2, tag);
  {
    const int g = l >> 4;
    h8 xf[8], yf[8];
#pragma unroll
    for (int k4 = 0; k4 < 4; ++k4) {
      const char* xa = sm + (xbase ^ (k4 << 6));
      const char* ya = sm + (ybase ^ (k4 << 6));
      xf[k4] = *(const h8*)xa; xf[4 + k4] = *(const h8*)(xa + 256);
      yf[k4] = *(const h8*)ya; yf[4 + k4] = *(const h8*)(ya + 256);
    }
    lbar();
    f4 acc0 = {0.f, 0.f, 0.f, 0.f}, acc1 = {0.f, 0.f, 0.f, 0.f};
#pragma unroll
    for (int kk = 0; kk < 4; ++kk) {
      acc0 = __builtin_amdgcn_mfma_f32_16x16x32_f16(xf[2 * kk], yf[2 * kk], acc0, 0, 0, 0);
      acc1 = __builtin_amdgcn_mfma_f32_16x16x32_f16(xf[2 * kk + 1], yf[2 * kk + 1], acc1, 0, 0, 0);
    }
    _Float16* Kc = (_Float16*)(sm + CV_U + 2560);
#pragma unroll
    for (int r = 0; r < 4; ++r) {
      const int i = 4 * g + r;
      const bool ok = (i >= ilo) && (i < ihi);
      Kc[ok ? kbase + 16 * i + (l & 15) : 648] = (_Float16)(acc0[r] + acc1[r]);
    }
    if (w >= 2 && l == 0) { ((float*)(sm + CV_U + 4096))[w - 2] = k2048; ((float*)(sm + CV_U + 4096))[2] = kscale; }
    if (tid == 0) Kc[656] = (_Float16)0.f;
  }
  lbar();
  const float scale = ((const float*)(sm + CV_U + 4096))[2];
  const float dsk = bf2f(dsk16);
  {
    const u16* Kc = (const u16*)(sm + CV_U + 2560);
    unsigned kv[8];
#pragma unroll
    for (int j = 0; j < 2; ++j)
#pragma unroll
      for (int e = 0; e < 4; ++e)
        kv[4 * j + e] = (unsigned)Kc[ebase[j] + ((epat[j] >> (4 * (7 - 2 * e))) & 15)] |
                        ((unsigned)Kc[ebase[j] + ((epat[j] >> (4 * (6 - 2 * e))) & 15)] << 16);
    if (tid == 0) {
      const float* kp = (const float*)(sm + CV_U + 4096);
      const unsigned kl = pk_h(kp[0] + kp[1], kp[0] + kp[1]);
      kv[0] = kl; kv[1] = kl;
    }
    unsigned f[7];
#pragma unroll
    for (int m = 0; m < 7; ++m) f[m] = __builtin_amdgcn_alignbit(kv[m + 1], kv[m], 16);
#pragma unroll
    for (int m = 0; m < 4; ++m) {
      *(u4*)(sm + (2 * m) * CV_KC + 16 * tid) = (u4){kv[m], kv[m + 1], kv[m + 2], kv[m + 3]};
      *(u4*)(sm + (2 * m + 1) * CV_KC + 16 * tid) = (u4){f[m], f[m + 1], f[m + 2], f[m + 3]};
    }
    if (tid < 24) *(u4*)(sm + (tid / 3) * CV_KC + 16 * (256 + tid % 3)) = (u4){0u, 0u, 0u, 0u};
    if (tid < 155) *(u4*)(sm + CV_U + tid * 16) = (u4){0u, 0u, 0u, 0u};
  }
  STAMP(xnt, 5);
  __builtin_amdgcn_s_setprio(3);
  if (w8 == 0) flag_wait(flg, (FL_A + (kb >> 5)) * 64 + l, l < 32, tag);
  lbar();
  STAMP(xnt, 6);
  {
    const u4 uv = __builtin_amdgcn_raw_buffer_load_b128(__builtin_amdgcn_make_buffer_rsrc(xnt, 0, SD * SL * 2, 0x00020000),
                                                        (d * SL + 8 * tid) * 2, 0, AUX_SC1_VOL);
    *(u4*)(sm + CV_U + (31 + (tid >> 2)) * 80 + (tid & 3) * 16) = uv;
  }
  lbar();
  STAMP(xnt, 8);
  __builtin_amdgcn_s_setprio(CVP);
  const int ks = w & 1, hb = w >> 1;
  const int b = l & 31, hh = l >> 5;
  const char* abase = sm + (7 - (b & 7)) * CV_KC + 16 * (255 - 4 * (16 * hb + 47) - (b >> 3) + 2 * ks + hh);
  const char* bbase = sm + CV_U + (31 - 16 * hb - 15 + b) * 80 + 32 * ks + 16 * hh;
  f16v t0 = {}, t1 = {};
#pragma unroll
  for (int i = 0; i < 16; ++i) {
    const h8 a_lo = *(const h8*)(abase + 64 * (15 - i) + 2048);
    const h8 a_hi = *(const h8*)(abase + 64 * (15 - i));
    const h8 b_lo = *(const h8*)(bbase + 80 * (15 - i));
    const h8 b_hi = *(const h8*)(bbase + 80 * (15 - i) + 2560);
    t0 = __builtin_amdgcn_mfma_f32_32x32x16_f16(a_lo, b_lo, t0, 0, 0, 0);
    t1 = __builtin_amdgcn_mfma_f32_32x32x16_f16(a_lo, b_hi, t1, 0, 0, 0);
    t1 = __builtin_amdgcn_mfma_f32_32x32x16_f16(a_hi, b_lo, t1, 0, 0, 0);
  }
  __builtin_amdgcn_s_setprio(3);
  lbar();
  STAMP(xnt, 9);
  f4 own[2];
  auto grp = [&](int G, int hf) -> f4 {
    const f16v& t = (G >> 1) ? t1 : t0; const int o = 8 * (G & 1) + 4 * hf;
    return (f4){t[o], t[o + 1], t[o + 2], t[o + 3]};
  };
  sfor<0, 4>([&](auto W) {
    if (w == W.v) {
#pragma unroll
      for (int s = 1; s < 4; ++s)
#pragma unroll
        for (int hf = 0; hf < 2; ++hf)
          *(f4*)(sm + ((((W.v + s) & 3) * 3 + (3 - s)) * 2 + hf) * 1024 + l * 16) = grp((W.v + s) & 3, hf);
      own[0] = grp(W.v, 0); own[1] = grp(W.v, 1);
    }
  });
  const int arow = 32 * (w >> 1) + b;
  const h8 uu = *(const h8*)(sm + CV_U + (31 + arow) * 80 + 32 * (w & 1) + 16 * hh);
  lbar();
#pragma unroll
  for (int sI = 0; sI < 3; ++sI)
#pragma unroll
    for (int hf = 0; hf < 2; ++hf) own[hf] += *(const f4*)(sm + ((w * 3 + sI) * 2 + hf) * 1024 + l * 16);
  {
    unsigned y0 = pk_bf(own[0][0] * scale, own[0][1] * scale), y1 = pk_bf(own[0][2] * scale, own[0][3] * scale);
    unsigned y2 = pk_bf(own[1][0] * scale, own[1][1] * scale), y3 = pk_bf(own[1][2] * scale, own[1][3] * scale);
    const auto ra = __builtin_amdgcn_permlane32_swap(y0, y2, false, false);
    const auto rb = __builtin_amdgcn_permlane32_swap(y1, y3, false, false);
    const unsigned yy[4] = {ra[0], rb[0], ra[1], rb[1]};
    u4 ov;
#pragma unroll
    for (int e = 0; e < 4; ++e) {
      const unsigned v = pk_bf(lo_bf(yy[e]) + bfr((float)uu[2 * e] * dsk), hi_bf(yy[e]) + bfr((float)uu[2 * e + 1] * dsk));
      ov[e] = v;
    }
    *(u4*)(sm + 24576 + arow * 80 + 32 * (w & 1) + 16 * hh) = ov;
  }
  lbar();
  st16(at, SD * SL * 2, (d * SL + 8 * tid) * 2, *(const u4*)(sm + 24576 + (tid >> 2) * 80 + (tid & 3) * 16));
  STAMP(xnt, 10);
  asm volatile("s_waitcnt vmcnt(0)" ::: "memory");
  bar();
  if (threadIdx.x == 0) flag_set(flg, FL_C + (kb >> 5), kb & 31, tag);
}

#define G2_NB 3

DEVI void g2_role(int bid, char* sm0, const u16* __restrict__ x, u16* hfr, u16* w2f, u16* at, u16* __restrict__ out,
                  unsigned* flg, unsigned tag) {
  __builtin_amdgcn_s_setprio(1);
  const int l = threadIdx.x & 63;
  const int w8 = __builtin_amdgcn_readfirstlane(threadIdx.x >> 6);
  const int w = w8 & 3, gq = w8 >> 2;
  const int xcd = bid & 7, idx = bid >> 3;
  const int pm = (xcd >> 1) * 8 + (idx >> 2), pn = (xcd & 1) * 4 + (idx & 3);
  const int t0 = pm * 64, d0 = pn * 64 + 32 * gq;
  const int g = l >> 4, li = l & 15, q = li >> 2, p = l & 3;
  const int k0 = 11 * w;
  char* sm = sm0 + gq * 24576;
  if (w8 == 0) flag_wait(flg, l < 8 ? (FL_A2 + pm) * 64 + l : (FL_W + (pn >> 1)) * 64 + (l - 8), l < 8 + NKP, tag);
  bar();
  STAMP((char*)hfr - WS_H, 7);
  const __amdgpu_buffer_rsrc_t rh = __builtin_amdgcn_make_buffer_rsrc((void*)hfr, 0, 128 * NKP * 1024, 0x00020000);
  const __amdgpu_buffer_rsrc_t rw = __builtin_amdgcn_make_buffer_rsrc((void*)w2f, 0, NKP * 32768, 0x00020000);
  const int lo = l * 16;
  int hso[4];
#pragma unroll
  for (int s = 0; s < 4; ++s) hso[s] = ((pm * 4 + ((w + s) & 3)) * NKP + k0) * 1024;
  const int wso = (k0 * 32 + pn * 4 + 2 * gq) * 1024;
  u4 hf[G2_NB][4], wf[G2_NB][2];
  const int rot = (((idx >> 2) + 3 * (idx & 3)) * 23) >> 4;
  auto stp = [&](int i) { int v = i + rot; v = v >= 22 ? v - 22 : v; return v >= 11 ? v - 11 : v; };
  auto ldw = [&](int buf, int i) {
    const int so = wso + stp(i) * 32768;
#pragma unroll
    for (int j = 0; j < 2; ++j) wf[buf][j] = __builtin_amdgcn_raw_buffer_load_b128(rw, lo + j * 1024, so, 16);
  };
  auto ldh = [&](int buf, int i, int s) { hf[buf][s] = __builtin_amdgcn_raw_buffer_load_b128(rh, lo, hso[s] + stp(i) * 1024, 16); };
  f4 acc[2][4];
#pragma unroll
  for (int j = 0; j < 2; ++j)
#pragma unroll
    for (int s = 0; s < 4; ++s) acc[j][s] = (f4){0.f, 0.f, 0.f, 0.f};
#pragma unroll
  for (int i = 0; i < G2_NB; ++i) {
    ldw(i, i);
#pragma unroll
    for (int s = 0; s < 4; ++s) ldh(i, i, s);
    __builtin_amdgcn_sched_barrier(0);
  }
  u2 xr[2];
  sfor<0, 11>([&](auto I) {
    constexpr int i = I.v, b = i % G2_NB;
#pragma unroll
    for (int s = 0; s < 4; ++s) {
#pragma unroll
      for (int j = 0; j < 2; ++j)
        acc[j][s] = __builtin_amdgcn_mfma_f32_16x16x32_bf16(__builtin_bit_cast(b8, wf[b][j]), __builtin_bit_cast(b8, hf[b][s]), acc[j][s], 0, 0, 0);
      __builtin_amdgcn_sched_barrier(0);
      if constexpr (i + G2_NB < 11) { ldh(b, i + G2_NB, s); __builtin_amdgcn_sched_barrier(0); }
    }
    if constexpr (i + G2_NB < 11) { ldw(b, i + G2_NB); __builtin_amdgcn_sched_barrier(0); }
    if constexpr (i == 8) {
#pragma unroll
      for (int j = 0; j < 2; ++j) xr[j] = *(const u2*)(x + (size_t)(t0 + 16 * w + li) * SD + d0 + 16 * j + 4 * g);
      __builtin_amdgcn_sched_barrier(0);
    }
  });
#pragma unroll
  for (int s = 1; s < 4; ++s)
#pragma unroll
    for (int j = 0; j < 2; ++j) *(f4*)(sm + ((((w + s) & 3) * 3 + (3 - s)) * 2 + j) * 1024 + l * 16) = acc[j][s];
  PIN(acc[0][0]); PIN(acc[1][0]);
  STAMP((char*)hfr - WS_H, 8);
  if (w8 == 0) flag_wait(flg, (FL_C + pn) * 64 + l, l < 32, tag);
  lbar();
  STAMP((char*)hfr - WS_H, 9);
  const u4 av = __builtin_amdgcn_raw_buffer_load_b128(__builtin_amdgcn_make_buffer_rsrc((void*)at, 0, SD * SL * 2, 0x00020000),
                                                      ((d0 + (l >> 1)) * SL + t0 + 16 * w + 8 * (l & 1)) * 2, 0, AUX_SC1_VOL);
  f4 v[2];
#pragma unroll
  for (int j = 0; j < 2; ++j) {
    v[j] = acc[j][0];
#pragma unroll
    for (int sI = 0; sI < 3; ++sI) v[j] += *(const f4*)(sm + ((w * 3 + sI) * 2 + j) * 1024 + l * 16);
  }
  asm volatile("s_waitcnt lgkmcnt(0)" ::: "memory");
  char* myR = sm + w * 6144;
  *(u4*)(myR + l * 16) = av;
  asm volatile("s_waitcnt lgkmcnt(0)" ::: "memory");
  STAMP((char*)hfr - WS_H, 10);
  u2 ov[2];
#pragma unroll
  for (int j = 0; j < 2; ++j) {
    const b4 a4 = trread(myR + (16 * j + 4 * g + q) * 32 + 8 * p);
    const u2 xv = xr[j];
    const float xs[4] = {lo_bf(xv.x), hi_bf(xv.x), lo_bf(xv.y), hi_bf(xv.y)};
    float o[4];
#pragma unroll
    for (int r = 0; r < 4; ++r) o[r] = bfr(xs[r] + (float)a4[r]) + bfr(v[j][r]);
    ov[j] = (u2){pk_bf(o[0], o[1]), pk_bf(o[2], o[3])};
  }
  st16(out, SL * SD * 2, ((t0 + 16 * w + li) * SD + d0 + 16 * (g & 1) + 8 * (g >> 1)) * 2, pair16(ov[0], ov[1]));
}

constexpr int ALL_LDS = G1_LDS > KC_LDS ? G1_LDS : KC_LDS;

__global__ __launch_bounds__(512, 4) void k_all(const u16* x, const u16* Are, const u16* Aim, const u16* Bre,
                                                const u16* Bim, const u16* Cre, const u16* Cim, const u16* logdt,
                                                const u16* dskip, const u16* w1, const u16* w2, char* ws, u16* out) {
  extern __shared__ __attribute__((aligned(16))) char sm[];
  const int bid = blockIdx.x;
  unsigned* flg = (unsigned*)(ws + WS_FLG);
  STAMP(ws, 0);
  const unsigned epoch = __hip_atomic_load((GAS unsigned*)flg, __ATOMIC_RELAXED, __HIP_MEMORY_SCOPE_AGENT);
  const unsigned tag = epoch + 0x9E3779B1u;
  if (bid >= 256) {
    g1_role(bid - 256, sm, x, w1, (u16*)(ws + WS_H), (_Float16*)(ws + WS_XNT), flg, tag);
    STAMP(ws, 1);
    g2_role(bid - 256, sm, x, (u16*)(ws + WS_H), (u16*)(ws + WS_W2F), (u16*)(ws + WS_AT), out, flg, tag);
    if (bid == 256 && threadIdx.x < 64) {
#pragma unroll 1
      for (int ln = 0; ln < 8; ++ln) flag_wait(flg, (threadIdx.x < 32 ? FL_A + ln : FL_C + ln) * 64 + (threadIdx.x & 31), true, tag);
      if (threadIdx.x == 0) flag_set(flg, 0, 0, epoch + 1u);
    }
  } else {
    kc_role(bid, sm, Are, Aim, Bre, Bim, Cre, Cim, logdt, w2, dskip, (_Float16*)(ws + WS_XNT), (u16*)(ws + WS_W2F),
            (u16*)(ws + WS_AT), flg, tag);
  }
  STAMP(ws, 3);
}

static void launch_all(void* const* d_in, void* d_out, void* d_ws, hipStream_t stream) {
  (void)hipFuncSetAttribute((const void*)k_all, hipFuncAttributeMaxDynamicSharedMemorySize, ALL_LDS);
  k_all<<<512, 512, ALL_LDS, stream>>>((const u16*)d_in[0], (const u16*)d_in[1], (const u16*)d_in[2], (const u16*)d_in[3],
                                       (const u16*)d_in[4], (const u16*)d_in[5], (const u16*)d_in[6], (const u16*)d_in[7],
                                       (const u16*)d_in[8], (const u16*)d_in[10], (const u16*)d_in[11], (char*)d_ws, (u16*)d_out);
}

extern "C" void kernel_launch(void* const* d_in, const int* in_sizes, int n_in, void* d_out, int out_size,
                              void* d_ws, size_t ws_size, hipStream_t stream) {
  launch_all(d_in, d_out, d_ws, stream);
}
```

```cpp
#include <hip/hip_runtime.h>
#include <hip/hip_bf16.h>
#include <stdint.h>

typedef unsigned short u16;
typedef _Float16 h8 __attribute__((ext_vector_type(8)));
typedef __bf16 b8 __attribute__((ext_vector_type(8)));
typedef __bf16 b4 __attribute__((ext_vector_type(4)));
typedef __bf16 b2 __attribute__((ext_vector_type(2)));
typedef float f4 __attribute__((ext_vector_type(4)));
typedef float f16v __attribute__((ext_vector_type(16)));
typedef unsigned u4 __attribute__((ext_vector_type(4)));
typedef unsigned u2 __attribute__((ext_vector_type(2)));
#define LAS __attribute__((address_space(3)))
#define GAS __attribute__((address_space(1)))
#define DEVI __device__ __forceinline__

constexpr int SL = 2048, SD = 512, SN = 128, SF = 1376;
constexpr int KSTR = 2112;
constexpr int NKS = SF / 32;
constexpr int NKP = NKS + 1;
constexpr size_t WS_XNT = 0;
constexpr size_t WS_KREV = WS_XNT + (size_t)SD * SL * 2;
constexpr size_t WS_KSC = WS_KREV + (size_t)SD * KSTR * 2;
constexpr int CNT_STR = 1088;
constexpr size_t WS_CNT = WS_KSC + 4096;
constexpr size_t WS_H = WS_CNT + 64 * CNT_STR * 4;
constexpr size_t WS_AT = WS_H + (size_t)128 * NKP * 1024;
constexpr size_t WS_W2F = WS_AT + (size_t)SD * SL * 2;

DEVI float bf2f(u16 v) { return __uint_as_float(((unsigned)v) << 16); }
DEVI float bfr(float f) { return (float)(__bf16)f; }
DEVI unsigned pk_bf(float a, float b) { b2 v = {(__bf16)a, (__bf16)b}; return __builtin_bit_cast(unsigned, v); }
DEVI unsigned pk_h(float a, float b) { unsigned r; asm("v_cvt_pk_f16_f32 %0, %1, %2" : "=v"(r) : "v"(a), "v"(b)); return r; }
DEVI float lo_bf(unsigned v) { return __uint_as_float(v << 16); }
DEVI float hi_bf(unsigned v) { return __uint_as_float(v & 0xffff0000u); }
template <int N> DEVI void wait_vm() { asm volatile("s_waitcnt vmcnt(%0) lgkmcnt(0)" ::"n"(N) : "memory"); }
DEVI void bar() { asm volatile("" ::: "memory"); __builtin_amdgcn_s_barrier(); asm volatile("" ::: "memory"); }
DEVI void lbar() { asm volatile("s_waitcnt lgkmcnt(0)" ::: "memory"); __builtin_amdgcn_s_barrier(); asm volatile("" ::: "memory"); }
DEVI void glds16(const void* g, char* l) {
  __builtin_amdgcn_global_load_lds((const GAS void*)g, (LAS void*)l, 16, 0, 0);
}
DEVI b4 trread(const char* p) { return __builtin_amdgcn_ds_read_tr16_b64_v4bf16((LAS b4*)p); }
DEVI b8 cat(b4 a, b4 b) { return __builtin_shufflevector(a, b, 0, 1, 2, 3, 4, 5, 6, 7); }
template <int V> struct IC { static constexpr int v = V; };
template <int I, int N, class F> DEVI void sfor(F&& f) { if constexpr (I < N) { f(IC<I>{}); sfor<I + 1, N>(f); } }
DEVI void st16(void* base, unsigned bytes, unsigned off, u4 v) {
  __builtin_amdgcn_raw_buffer_store_b128(v, __builtin_amdgcn_make_buffer_rsrc(base, 0, bytes, 0x00020000), off, 0, 16);
}
DEVI u4 pair16(u2 a, u2 b) {
  const auto rx = __builtin_amdgcn_permlane16_swap(a.x, b.x, false, false);
  const auto ry = __builtin_amdgcn_permlane16_swap(a.y, b.y, false, false);
  return (u4){rx[0], ry[0], rx[1], ry[1]};
}
template <int CTRL, int RM> DEVI float dppf(float v) {
  return __builtin_bit_cast(float, __builtin_amdgcn_update_dpp(0, __builtin_bit_cast(int, v), CTRL, RM, 0xf, false));
}
DEVI float wave_max(float v) {
  v = fmaxf(v, dppf<0x111, 0xf>(v)); v = fmaxf(v, dppf<0x112, 0xf>(v)); v = fmaxf(v, dppf<0x114, 0xf>(v));
  v = fmaxf(v, dppf<0x118, 0xf>(v)); v = fmaxf(v, dppf<0x142, 0xa>(v)); v = fmaxf(v, dppf<0x143, 0xc>(v));
  return __builtin_bit_cast(float, __builtin_amdgcn_readlane(__builtin_bit_cast(int, v), 63));
}
DEVI float wave_sum(float v) {
  v += dppf<0x111, 0xf>(v); v += dppf<0x112, 0xf>(v); v += dppf<0x114, 0xf>(v);
  v += dppf<0x118, 0xf>(v); v += dppf<0x142, 0xa>(v); v += dppf<0x143, 0xc>(v);
  return __builtin_bit_cast(float, __builtin_amdgcn_readlane(__builtin_bit_cast(int, v), 63));
}
#define PIN(x) asm volatile("" : "+v"(x))
__device__ const unsigned g_ones[4] = {0x3f803f80u, 0x3f803f80u, 0x3f803f80u, 0x3f803f80u};

constexpr int G1_S = 5;
constexpr int G1_STAGE = 16384;
constexpr int G1_LDS = G1_S * G1_STAGE;

template <int WM>
DEVI void g1_body(int bid, char* sm, const u16* __restrict__ x, const u16* __restrict__ w1,
                  u16* __restrict__ h, _Float16* __restrict__ xnt, int w) {
  const int tid = threadIdx.x, l = tid & 63;
  const int wn = w & 3;
  const int xcd = bid & 7, idx = bid >> 3;
  const int pm = (xcd >> 1) * 8 + (idx >> 2), pn = (xcd & 1) * 4 + (idx & 3);
  const int r0 = pm * 64, c0 = pn * 176;
  const int g = l >> 4, li = l & 15, q = li >> 2, p = l & 3;

  const char* ga[2];
  unsigned gs[2];
#pragma unroll
  for (int j = 0; j < 2; ++j) {
    const int pc = 2 * w + j;
    if (pc < 4) {
      const int xrow = 16 * pc + (l >> 2);
      ga[j] = (const char*)(x + (size_t)(r0 + xrow) * SD + (((l & 3) ^ ((xrow >> 2) & 3)) * 8));
      gs[j] = 64;
    } else {
      const int ci = (pc - 4) * 64 + l, k = ci / 24, cp = ci - k * 24;
      const int c = cp ^ (((k >> 1) & 3) << 1);
      int col = c0 + 8 * c; if (col > SF - 8) col = SF - 8;
      ga[j] = c >= 22 ? (const char*)g_ones : (const char*)(w1 + (size_t)k * SF + col);
      gs[j] = c >= 22 ? 0 : 32 * SF * 2;
    }
  }
  char* const lb = sm + w * 2048;
  unsigned kc = (2 * (idx >> 2) + 4 * (idx & 3)) & 15;
  auto issue = [&](int slot, int j) {
    glds16(ga[j] + (unsigned long)kc * gs[j], lb + slot * G1_STAGE + j * 1024);
    if (j == 1) kc = (kc + 1) & 15;
  };
#pragma unroll
  for (int s = 0; s < G1_S; ++s) { issue(s, 0); issue(s, 1); }

  const int mt0 = 2 * WM + (wn & 1), mt1 = 2 * WM + ((wn & 1) ^ 1);
  const int xo0 = li * 64 + (((g >> 1) ^ q) * 16) + 8 * (g & 1), xo1 = li * 64 + (((2 + (g >> 1)) ^ q) * 16) + 8 * (g & 1);
  const char* xa00 = sm + mt0 * 1024 + xo0; const char* xa01 = sm + mt0 * 1024 + xo1;
  const char* xa10 = sm + mt1 * 1024 + xo0; const char* xa11 = sm + mt1 * 1024 + xo1;
  const char* wa[3];
#pragma unroll
  for (int j = 0; j < 3; ++j) {
    const int row = 4 * g + q;
    wa[j] = sm + 4096 + row * 384 + (((3 * wn + j) ^ ((row >> 1) & 3)) * 32) + 8 * p;
  }
  const int pi = tid >> 4, c4 = tid & 15;
  unsigned hoff[3];
#pragma unroll
  for (int j = 0; j < 3; ++j) {
    const int col = c0 + 16 * (3 * wn + j) + 8 * (g >> 1);
    const bool ok = (3 * wn + j < 11);
    hoff[j] = ok ? (unsigned)((((pm * 4 + ((g & 1) ? mt1 : mt0)) * NKP + (col >> 5)) * 64 + ((col >> 3) & 3) * 16 + li) * 16) : 0x80000000u;
    PIN(hoff[j]);
  }
  u2 xva, xvb;

  f4 acc[2][3], cs[3], rq = {0.f, 0.f, 0.f, 0.f};
#pragma unroll
  for (int j = 0; j < 3; ++j) { acc[0][j] = acc[1][j] = cs[j] = (f4){0.f, 0.f, 0.f, 0.f}; }
  const b8 ones = {(__bf16)1.0f, (__bf16)1.0f, (__bf16)1.0f, (__bf16)1.0f, (__bf16)1.0f, (__bf16)1.0f, (__bf16)1.0f, (__bf16)1.0f};
  b8 xf[2][2], wf[2][3];
  auto rd = [&](int buf, int so) {
#pragma unroll
    for (int j = 0; j < 3; ++j) wf[buf][j] = cat(trread(wa[j] + so), trread(wa[j] + so + 16 * 384));
    xf[buf][0] = cat(*(const b4*)(xa00 + so), *(const b4*)(xa01 + so));
    xf[buf][1] = cat(*(const b4*)(xa10 + so), *(const b4*)(xa11 + so));
  };
  wait_vm<2 * (G1_S - 1)>();
  bar();
  rd(0, 0);
  sfor<0, 16>([&](auto K) {
    constexpr int k = K.v, u = k & 1;
    if constexpr (k < 15) {
      constexpr int later = ((k + G1_S) < 16 ? (k + G1_S) : 16) - (k + 2);
      wait_vm<2 * later + (k > 11 ? 2 : 0)>();
      bar();
      if constexpr (k + G1_S < 16) issue(k % G1_S, 0);
      if constexpr (k == 11) {
        xva = *(const u2*)(x + (size_t)(r0 + 2 * pi) * SD + 64 * pn + 4 * c4);
        xvb = *(const u2*)(x + (size_t)(r0 + 2 * pi + 1) * SD + 64 * pn + 4 * c4);
      }
      rd(u ^ 1, ((k + 1) % G1_S) * G1_STAGE);
    }
    __builtin_amdgcn_sched_barrier(0);
#pragma unroll
    for (int j = 0; j < 3; ++j) acc[0][j] = __builtin_amdgcn_mfma_f32_16x16x32_bf16(wf[u][j], xf[u][0], acc[0][j], 0, 0, 0);
    rq = __builtin_amdgcn_mfma_f32_16x16x32_bf16(xf[u][0], xf[u][0], rq, 0, 0, 0);
    __builtin_amdgcn_sched_barrier(0);
    if constexpr (k + G1_S < 16) issue(k % G1_S, 1);
    __builtin_amdgcn_sched_barrier(0);
#pragma unroll
    for (int j = 0; j < 3; ++j) acc[1][j] = __builtin_amdgcn_mfma_f32_16x16x32_bf16(wf[u][j], xf[u][1], acc[1][j], 0, 0, 0);
    if constexpr (u == WM) {
#pragma unroll
      for (int j = 0; j < 3; ++j) cs[j] = __builtin_amdgcn_mfma_f32_16x16x32_bf16(wf[u][j], ones, cs[j], 0, 0, 0);
    }
    __builtin_amdgcn_sched_barrier(0);
  });
  PIN(xva); PIN(xvb);
  float* raw = (float*)(sm + 2 * G1_STAGE);
  float2* stats = (float2*)(sm + 2 * G1_STAGE + 1024);
  float* csum = (float*)(sm + 2 * G1_STAGE + 2048);
  if (wn == 3 && g == 0) { raw[16 * mt0 + li] = acc[0][2][0]; raw[16 * mt1 + li] = acc[1][2][0]; }
  if (wn < 2 && g == q) raw[64 + 16 * mt0 + li] = p == 0 ? rq[0] : (p == 1 ? rq[1] : (p == 2 ? rq[2] : rq[3]));
  if (li == 0) {
#pragma unroll
    for (int j = 0; j < 3; ++j) *(f4*)(csum + WM * 192 + 16 * (3 * wn + j) + 4 * g) = cs[j];
  }
  lbar();
  if (tid < 64) {
    const float mean = raw[tid] * (1.0f / SD);
    float var = raw[64 + tid] * (1.0f / SD) - mean * mean; var = var < 0.f ? 0.f : var;
    stats[tid] = make_float2(bfr(mean), bfr(__builtin_amdgcn_sqrtf(bfr(bfr(var) + 1e-5f))));
  }
  f4 csr[3];
#pragma unroll
  for (int j = 0; j < 3; ++j)
    csr[j] = *(const f4*)(csum + 16 * (3 * wn + j) + 4 * g) + *(const f4*)(csum + 192 + 16 * (3 * wn + j) + 4 * g);
  lbar();
  unsigned* T = (unsigned*)(sm + 3 * G1_STAGE);
  {
    const float2 s0 = stats[2 * pi], s1 = stats[2 * pi + 1];
    const float i0 = __builtin_amdgcn_rcpf(s0.y), i1 = __builtin_amdgcn_rcpf(s1.y);
#pragma unroll
    for (int e = 0; e < 4; ++e) {
      const unsigned wva = xva[e >> 1], wvb = xvb[e >> 1];
      const float a = (e & 1) ? hi_bf(wva) : lo_bf(wva), b = (e & 1) ? hi_bf(wvb) : lo_bf(wvb);
      const float na = bfr(bfr(a - s0.x) * i0), nb = bfr(bfr(b - s1.x) * i1);
      T[(4 * c4 + e) * 32 + (pi ^ ((c4 >> 1) << 2))] = pk_h(na, nb);
    }
  }
  {
    const float2 st0 = stats[16 * mt0 + li], st1 = stats[16 * mt1 + li];
    const float inv0 = __builtin_amdgcn_rcpf(st0.y), inv1 = __builtin_amdgcn_rcpf(st1.y);
#pragma unroll
    for (int j = 0; j < 3; ++j) {
      float v[4], u[4];
#pragma unroll
      for (int r = 0; r < 4; ++r) {
        v[r] = fmaxf((acc[0][j][r] - st0.x * csr[j][r]) * inv0, 0.f);
        u[r] = fmaxf((acc[1][j][r] - st1.x * csr[j][r]) * inv1, 0.f);
      }
      u4 o = pair16((u2){pk_bf(v[0], v[1]), pk_bf(v[2], v[3])}, (u2){pk_bf(u[0], u[1]), pk_bf(u[2], u[3])});
      if (c0 + 16 * (3 * wn + j) >= SF) o = (u4){0u, 0u, 0u, 0u};
      st16(h, 128 * NKP * 1024, hoff[j], o);
    }
  }
  {
    lbar();
    const int ch = tid >> 3, m = tid & 7;
    const u4 v = *(const u4*)(T + ch * 32 + 4 * (m ^ (ch >> 3)));
    st16(xnt, SD * SL * 2, ((64 * pn + ch) * SL + r0 + 8 * m) * 2, v);
  }
}

DEVI void g1_role(int bid, char* sm, const u16* __restrict__ x, const u16* __restrict__ w1,
                  u16* __restrict__ h, _Float16* __restrict__ xnt) {
  const int w = __builtin_amdgcn_readfirstlane(threadIdx.x >> 6);
  __builtin_amdgcn_s_setprio(2);
  if (w < 4) g1_body<0>(bid, sm, x, w1, h, xnt, w); else g1_body<1>(bid, sm, x, w1, h, xnt, w);
}

#define KGP 0
constexpr int KG_CH = 80 * 512;
constexpr int KG_LDS = 2 * KG_CH;

struct cf { float r, i; };
DEVI cf cmul(cf a, cf b) {
  float t, u, re, im;
  asm("v_mul_f32 %0, %2, %3\n\tv_mul_f32 %1, %2, %4" : "=&v"(t), "=&v"(u) : "v"(a.i), "v"(b.i), "v"(b.r));
  asm("v_fma_f32 %0, %2, %3, -%4\n\tv_fma_f32 %1, %2, %5, %6" : "=&v"(re), "=&v"(im) : "v"(a.r), "v"(b.r), "v"(t), "v"(b.i), "v"(u));
  return {re, im};
}
DEVI cf cseed(float al, float om, float m) {
  const float mag = __builtin_amdgcn_exp2f((al * m) * 1.44269504088896341f);
  const float ph = om * m;
  const float chi = 0.15915494f, clo = 6.4206382e-9f;
  const float fr = __builtin_fmaf(ph, clo, __builtin_fmaf(ph, chi, -__builtin_rintf(ph * chi)));
  return {mag * __builtin_amdgcn_cosf(fr), mag * __builtin_amdgcn_sinf(fr)};
}

DEVI void kg_role(int kb, char* sm0, const u16* __restrict__ Are, const u16* __restrict__ Aim,
                  const u16* __restrict__ Bre, const u16* __restrict__ Bim, const u16* __restrict__ Cre,
                  const u16* __restrict__ Cim, const u16* __restrict__ logdt, const u16* __restrict__ w2,
                  _Float16* __restrict__ krev, float* __restrict__ ksc, u16* __restrict__ w2f, u16* __restrict__ at) {
  const int l = threadIdx.x & 63;
  const int w8 = __builtin_amdgcn_readfirstlane(threadIdx.x >> 6);
  const int w = w8 & 3, tid = threadIdx.x & 255;
  const int d = 2 * kb + (w8 >> 2);
  char* sm = sm0 + (w8 >> 2) * KG_CH;
  const int n = tid & 127;
  __builtin_amdgcn_s_setprio(3);
  const u16 ldt = logdt[d], are = Are[d * SN + n], aim = Aim[d * SN + n];
  u16 pbr[2], pbi[2], pcr[2], pci[2];
#pragma unroll
  for (int hh = 0; hh < 2; ++hh) {
    const int nn = d * SN + (n & 63) + 64 * hh;
    pbr[hh] = Bre[nn]; pbi[hh] = Bim[nn]; pcr[hh] = Cre[nn]; pci[hh] = Cim[nn];
  }
  __builtin_amdgcn_sched_barrier(0);
  const int fr = kb * 8 + w8;
  u16 wv[8] = {0, 0, 0, 0, 0, 0, 0, 0};
  if (fr < NKS * 32) {
    const u16* src = w2 + (size_t)(32 * (fr >> 5) + 8 * (l >> 4)) * SD + 16 * (fr & 31) + (l & 15);
#pragma unroll
    for (int e = 0; e < 8; ++e) wv[e] = src[(size_t)e * SD];
  }
  st16(at, SD * SL * 2, (kb * 512 + threadIdx.x) * 16, (u4){0xffffffffu, 0xffffffffu, 0xffffffffu, 0xffffffffu});
  __builtin_amdgcn_s_setprio(KGP);
  int xr, yr;
  {
    const int li = l & 15, y = li << w;
    yr = y < 16 ? y : (y < 32 ? 8 + (y >> 1) : (y < 64 ? 16 + (y >> 2) : 24 + (y >> 3)));
    xr = 40 + (w == 0 ? li : (w == 3 ? 32 + (li & 7) : 16 + li));
  }
  const int xbase = xr * 512 + (((l >> 4) ^ (xr & 15)) << 4), ybase = yr * 512 + (((l >> 4) ^ (yr & 15)) << 4);
  int ebase; unsigned epat;
  {
    const int m = 255 - tid;
    if (m < 32) { ebase = 8 * m; epat = 0x76543210u; }
    else if (m < 64) { ebase = 128 + 4 * m; epat = 0x43222100u; }
    else if (m < 128) { ebase = 256 + 2 * m; epat = 0x22111000u; }
    else { ebase = 384 + m; epat = (m & 1) ? 0x11110000u : 0x11100000u; }
  }
  const int kbase = w == 0 ? 0 : (w == 3 ? 512 : 256), ilo = w == 2 ? 8 : 0, ihi = (w & 1) ? 8 : 16;
  const float dt = bfr(expf(bf2f(ldt)));
  const float al = dt * bf2f(are), om = dt * bf2f(aim);
  const int wa = (n >> 2) * 16 + (n & 3) * 4;
#define KG_ST(row, re, im) *(unsigned*)(sm + (row) * 512 + (wa ^ (((row) & 15) << 4))) = pk_h(re, im)
  float k2048 = 0.f;
  if (w < 2) {
    cf P[16], Q[8], R[8];
    const cf e1 = cseed(al, om, 1.f), e16 = cseed(al, om, 16.f), e32 = cseed(al, om, 32.f), e64 = cseed(al, om, 64.f);
    P[0] = {1.f, 0.f}; P[1] = e1;
#pragma unroll
    for (int j = 2; j < 16; ++j) P[j] = cmul(P[j >> 1], P[j - (j >> 1)]);
#pragma unroll
    for (int j = 0; j < 16; ++j) KG_ST(j, P[j].r, P[j].i);
    Q[0] = e16;
#pragma unroll
    for (int m = 1; m < 8; ++m) Q[m] = cmul(e16, P[2 * m]);
#pragma unroll
    for (int m = 0; m < 8; ++m) KG_ST(16 + m, Q[m].r, Q[m].i);
    R[0] = e32;
#pragma unroll
    for (int m = 1; m < 8; ++m) R[m] = cmul(e32, m < 4 ? P[4 * m] : Q[2 * (m - 4)]);
#pragma unroll
    for (int m = 0; m < 8; ++m) KG_ST(24 + m, R[m].r, R[m].i);
    KG_ST(32, e64.r, e64.i);
#pragma unroll
    for (int m = 1; m < 8; ++m) {
      const cf t = cmul(e64, m < 2 ? P[8 * m] : (m < 4 ? Q[4 * (m - 2)] : R[2 * (m - 4)]));
      KG_ST(32 + m, t.r, t.i);
    }
  } else {
    cf cb[2];
    float mx = 0.f;
#pragma unroll
    for (int hh = 0; hh < 2; ++hh) {
      const float br = dt * bf2f(pbr[hh]), bi = dt * bf2f(pbi[hh]);
      const float cr = bf2f(pcr[hh]), ci = bf2f(pci[hh]);
      cb[hh] = cmul({cr, ci}, {br, bi});
      mx = fmaxf(mx, fmaxf(fabsf(cb[hh].r), fabsf(cb[hh].i)));
    }
    const float on = -om;
    const cf e16 = cseed(al, on, 16.f), e32 = cseed(al, on, 32.f), e64 = cseed(al, on, 64.f), e128 = cseed(al, on, 128.f);
    const cf e256 = cseed(al, on, 256.f), e512 = cseed(al, on, 512.f), e1024 = cseed(al, on, 1024.f);
    mx = wave_max(mx);
    int ex = (mx > 0.f && mx < 3.0e38f) ? __builtin_amdgcn_frexp_expf(mx) : 0;
    ex = ex < -100 ? -100 : (ex > 100 ? 100 : ex);
    const float sc = __builtin_amdgcn_ldexpf(1.0f, -ex);
    if (tid == 128) ksc[d] = __builtin_amdgcn_ldexpf(1.0f, ex);
    const cf c0 = (w == 2) ? cb[0] : cb[1];
    const cf cbs = {c0.r * sc, -c0.i * sc};
    cf a0 = cbs, a0b = cmul(cbs, e128), a1 = cmul(cbs, e256), a2 = cmul(cbs, e512), a3 = cmul(cbs, e1024);
#pragma unroll
    for (int i = 0; i < 8; ++i) {
      KG_ST(40 + i, a0.r, a0.i);
      KG_ST(48 + i, a0b.r, a0b.i);
      KG_ST(56 + i, a1.r, a1.i);
      KG_ST(64 + i, a2.r, a2.i);
      KG_ST(72 + i, a3.r, a3.i);
      if (i < 7) { a0 = cmul(a0, e16); a0b = cmul(a0b, e16); a1 = cmul(a1, e32); a2 = cmul(a2, e64); a3 = cmul(a3, e128); }
    }
    k2048 = wave_sum(a3.r * e128.r - a3.i * e128.i);
  }
#undef KG_ST
  if (fr < (NKS + 1) * 32) {
    const u4 f = {(unsigned)wv[0] | ((unsigned)wv[1] << 16), (unsigned)wv[2] | ((unsigned)wv[3] << 16),
                  (unsigned)wv[4] | ((unsigned)wv[5] << 16), (unsigned)wv[6] | ((unsigned)wv[7] << 16)};
    st16(w2f, (NKS + 1) * 32768, (fr * 64 + l) * 16, f);
  }
  lbar();
  {
    const int g = l >> 4;
    h8 xf[8], yf[8];
#pragma unroll
    for (int k4 = 0; k4 < 4; ++k4) {
      const char* xa = sm + (xbase ^ (k4 << 6));
      const char* ya = sm + (ybase ^ (k4 << 6));
      xf[k4] = *(const h8*)xa; xf[4 + k4] = *(const h8*)(xa + 256);
      yf[k4] = *(const h8*)ya; yf[4 + k4] = *(const h8*)(ya + 256);
    }
    lbar();
    f4 acc0 = {0.f, 0.f, 0.f, 0.f}, acc1 = {0.f, 0.f, 0.f, 0.f};
#pragma unroll
    for (int kk = 0; kk < 4; ++kk) {
      acc0 = __builtin_amdgcn_mfma_f32_16x16x32_f16(xf[2 * kk], yf[2 * kk], acc0, 0, 0, 0);
      acc1 = __builtin_amdgcn_mfma_f32_16x16x32_f16(xf[2 * kk + 1], yf[2 * kk + 1], acc1, 0, 0, 0);
    }
    _Float16* Kc = (_Float16*)sm;
#pragma unroll
    for (int r = 0; r < 4; ++r) {
      const int i = 4 * g + r;
      const bool ok = (i >= ilo) && (i < ihi);
      Kc[ok ? kbase + 16 * i + (l & 15) : 648] = (_Float16)(acc0[r] + acc1[r]);
    }
    if (w >= 2 && l == 0) ((float*)(sm + 1536))[w - 2] = k2048;
  }
  lbar();
  {
    const _Float16* Kc = (const _Float16*)sm + ebase;
    h8 o;
#pragma unroll
    for (int e = 0; e < 8; ++e) o[e] = Kc[(epat >> (4 * (7 - e))) & 15];
    if (tid == 0) {
      const float* kp = (const float*)(sm + 1536);
      const _Float16 klast = (_Float16)(kp[0] + kp[1]);
      o[0] = klast; o[1] = klast; o[2] = klast; o[3] = klast;
    }
    st16(krev, SD * KSTR * 2, (d * KSTR + 8 * tid) * 2, __builtin_bit_cast(u4, o));
    if (tid < 8) st16(krev, SD * KSTR * 2, (d * KSTR + 2048 + 8 * tid) * 2, (u4){0u, 0u, 0u, 0u});
  }
}

#define CVP 1
constexpr int CV_KC = 4160;
constexpr int CV_U = 8 * CV_KC;
constexpr int CV_CH = CV_U + 95 * 80;
constexpr int CV_LDS = CV_CH;

DEVI void cv_role(int d, char* sm, const _Float16* __restrict__ xnt, const _Float16* __restrict__ krev,
                  const float* __restrict__ ksc, const u16* __restrict__ dskip, u16* __restrict__ at,
                  unsigned* __restrict__ cnt) {
  __builtin_amdgcn_s_setprio(3);
  const int l = threadIdx.x & 63;
  const int w = __builtin_amdgcn_readfirstlane(threadIdx.x >> 6);
  const int tid = threadIdx.x;
  const u4 uv = *(const u4*)(xnt + (size_t)d * SL + 8 * tid);
  const u4* kg = (const u4*)(krev + (size_t)d * KSTR);
  const u4 k0 = kg[tid], k1 = kg[tid + 1];
  u4 k2 = {0u, 0u, 0u, 0u}, k3 = {0u, 0u, 0u, 0u};
  if (tid < 3) { k2 = kg[256 + tid]; k3 = kg[257 + tid]; }
  const float scale = ksc[d];
  const float dsk = bf2f(dskip[d]);
  if (tid < 155) *(u4*)(sm + CV_U + tid * 16) = (u4){0u, 0u, 0u, 0u};
  *(u4*)(sm + CV_U + (31 + (tid >> 2)) * 80 + (tid & 3) * 16) = uv;
  auto build = [&](u4 a, u4 b, int q) {
    const unsigned wv[8] = {a.x, a.y, a.z, a.w, b.x, b.y, b.z, b.w};
    unsigned f[7];
#pragma unroll
    for (int m = 0; m < 7; ++m) f[m] = __builtin_amdgcn_alignbit(wv[m + 1], wv[m], 16);
#pragma unroll
    for (int m = 0; m < 4; ++m) {
      *(u4*)(sm + (2 * m) * CV_KC + 16 * q) = (u4){wv[m], wv[m + 1], wv[m + 2], wv[m + 3]};
      *(u4*)(sm + (2 * m + 1) * CV_KC + 16 * q) = (u4){f[m], f[m + 1], f[m + 2], f[m + 3]};
    }
  };
  build(k0, k1, tid);
  if (tid < 3) build(k2, k3, 256 + tid);
  lbar();
  __builtin_amdgcn_s_setprio(CVP);
  const int ks = w & 1, hb = w >> 1;
  const int b = l & 31, hh = l >> 5;
  const char* abase = sm + (7 - (b & 7)) * CV_KC + 16 * (255 - 4 * (16 * hb + 47) - (b >> 3) + 2 * ks + hh);
  const char* bbase = sm + CV_U + (31 - 16 * hb - 15 + b) * 80 + 32 * ks + 16 * hh;
  f16v t0 = {}, t1 = {};
#pragma unroll
  for (int i = 0; i < 16; ++i) {
    const h8 a_lo = *(const h8*)(abase + 64 * (15 - i) + 2048);
    const h8 a_hi = *(const h8*)(abase + 64 * (15 - i));
    const h8 b_lo = *(const h8*)(bbase + 80 * (15 - i));
    const h8 b_hi = *(const h8*)(bbase + 80 * (15 - i) + 2560);
    t0 = __builtin_amdgcn_mfma_f32_32x32x16_f16(a_lo, b_lo, t0, 0, 0, 0);
    t1 = __builtin_amdgcn_mfma_f32_32x32x16_f16(a_lo, b_hi, t1, 0, 0, 0);
    t1 = __builtin_amdgcn_mfma_f32_32x32x16_f16(a_hi, b_lo, t1, 0, 0, 0);
  }
  __builtin_amdgcn_s_setprio(3);
  lbar();
  f4 own[2];
  auto grp = [&](int G, int hf) -> f4 {
    const f16v& t = (G >> 1) ? t1 : t0; const int o = 8 * (G & 1) + 4 * hf;
    return (f4){t[o], t[o + 1], t[o + 2], t[o + 3]};
  };
  sfor<0, 4>([&](auto W) {
    if (w == W.v) {
#pragma unroll
      for (int s = 1; s < 4; ++s)
#pragma unroll
        for (int hf = 0; hf < 2; ++hf)
          *(f4*)(sm + ((((W.v + s) & 3) * 3 + (3 - s)) * 2 + hf) * 1024 + l * 16) = grp((W.v + s) & 3, hf);
      own[0] = grp(W.v, 0); own[1] = grp(W.v, 1);
    }
  });
  const int arow = 32 * (w >> 1) + b;
  const h8 uu = *(const h8*)(sm + CV_U + (31 + arow) * 80 + 32 * (w & 1) + 16 * hh);
  lbar();
#pragma unroll
  for (int sI = 0; sI < 3; ++sI)
#pragma unroll
    for (int hf = 0; hf < 2; ++hf) own[hf] += *(const f4*)(sm + ((w * 3 + sI) * 2 + hf) * 1024 + l * 16);
  {
    unsigned y0 = pk_bf(own[0][0] * scale, own[0][1] * scale), y1 = pk_bf(own[0][2] * scale, own[0][3] * scale);
    unsigned y2 = pk_bf(own[1][0] * scale, own[1][1] * scale), y3 = pk_bf(own[1][2] * scale, own[1][3] * scale);
    const auto ra = __builtin_amdgcn_permlane32_swap(y0, y2, false, false);
    const auto rb = __builtin_amdgcn_permlane32_swap(y1, y3, false, false);
    const unsigned yy[4] = {ra[0], rb[0], ra[1], rb[1]};
    u4 ov;
#pragma unroll
    for (int e = 0; e < 4; ++e) {
      const unsigned v = pk_bf(lo_bf(yy[e]) + bfr((float)uu[2 * e] * dsk), hi_bf(yy[e]) + bfr((float)uu[2 * e + 1] * dsk));
      ov[e] = v == 0xffffffffu ? 0x7fc07fc0u : v;
    }
    *(u4*)(sm + 24576 + arow * 80 + 32 * (w & 1) + 16 * hh) = ov;
  }
  lbar();
  st16(at, SD * SL * 2, (d * SL + 8 * tid) * 2, *(const u4*)(sm + 24576 + (tid >> 2) * 80 + (tid & 3) * 16));
  bar();
  if (tid < 8)
    __hip_atomic_fetch_add((GAS unsigned*)(cnt + ((d >> 6) * 8 + tid) * CNT_STR), 1u, __ATOMIC_RELAXED, __HIP_MEMORY_SCOPE_AGENT);
}

constexpr int AUX_SC1_VOL = (int)0x80000010u;
constexpr int G2_LDS = 49152;
#define G2_NB 3

DEVI void g2_role(int bid, char* sm, const u16* __restrict__ x, const u16* __restrict__ hfr,
                  const u16* __restrict__ w2f, u16* at, unsigned* cnt, u16* __restrict__ out) {
  __builtin_amdgcn_s_setprio(1);
  const int tid = threadIdx.x, l = tid & 63;
  const int w = __builtin_amdgcn_readfirstlane(tid >> 6);
  const int xcd = bid & 7, idx = bid >> 3;
  const int pm = (xcd >> 1) * 8 + (idx >> 2), pn = (xcd & 1) * 4 + (idx & 3);
  const int t0 = pm * 64, d0 = pn * 64;
  const int g = l >> 4, li = l & 15, q = li >> 2, p = l & 3;
  const int k0 = 11 * w;

  const __amdgpu_buffer_rsrc_t rh = __builtin_amdgcn_make_buffer_rsrc((void*)hfr, 0, 128 * NKP * 1024, 0x00020000);
  const __amdgpu_buffer_rsrc_t rw = __builtin_amdgcn_make_buffer_rsrc((void*)w2f, 0, NKP * 32768, 0x00020000);
  const int lo = l * 16;
  int hso[4];
#pragma unroll
  for (int s = 0; s < 4; ++s) hso[s] = ((pm * 4 + ((w + s) & 3)) * NKP + k0) * 1024;
  const int wso = (k0 * 32 + pn * 4) * 1024;
  u4 hf[G2_NB][4], wf[G2_NB][4];
  const int rot = (((idx >> 2) + 3 * (idx & 3)) * 23) >> 4;
  auto stp = [&](int i) { int v = i + rot; v = v >= 22 ? v - 22 : v; return v >= 11 ? v - 11 : v; };
  auto ldw = [&](int buf, int i) {
    const int so = wso + stp(i) * 32768;
#pragma unroll
    for (int j = 0; j < 4; ++j) wf[buf][j] = __builtin_amdgcn_raw_buffer_load_b128(rw, lo + j * 1024, so, 0);
  };
  auto ldh = [&](int buf, int i, int s) { hf[buf][s] = __builtin_amdgcn_raw_buffer_load_b128(rh, lo, hso[s] + stp(i) * 1024, 0); };
  f4 acc[4][4];
#pragma unroll
  for (int j = 0; j < 4; ++j)
#pragma unroll
    for (int s = 0; s < 4; ++s) acc[j][s] = (f4){0.f, 0.f, 0.f, 0.f};
#pragma unroll
  for (int i = 0; i < G2_NB; ++i) {
    ldw(i, i);
#pragma unroll
    for (int s = 0; s < 4; ++s) ldh(i, i, s);
    __builtin_amdgcn_sched_barrier(0);
  }
  u2 xr[4];
  const __amdgpu_buffer_rsrc_t rs = __builtin_amdgcn_make_buffer_rsrc((void*)at, 0, SD * SL * 2, 0x00020000);
  sfor<0, 11>([&](auto I) {
    constexpr int i = I.v, b = i % G2_NB;
#pragma unroll
    for (int s = 0; s < 4; ++s) {
#pragma unroll
      for (int j = 0; j < 4; ++j)
        acc[j][s] = __builtin_amdgcn_mfma_f32_16x16x32_bf16(__builtin_bit_cast(b8, wf[b][j]), __builtin_bit_cast(b8, hf[b][s]), acc[j][s], 0, 0, 0);
      __builtin_amdgcn_sched_barrier(0);
      if constexpr (i + G2_NB < 11) { ldh(b, i + G2_NB, s); __builtin_amdgcn_sched_barrier(0); }
    }
    if constexpr (i + G2_NB < 11) { ldw(b, i + G2_NB); __builtin_amdgcn_sched_barrier(0); }
    if constexpr (i == 8) {
#pragma unroll
      for (int j = 0; j < 4; ++j) xr[j] = *(const u2*)(x + (size_t)(t0 + 16 * w + li) * SD + d0 + 16 * j + 4 * g);
      __builtin_amdgcn_sched_barrier(0);
    }
  });
  const int aoff = ((d0 + l) * SL + t0 + 16 * w) * 2;
  u4 av0 = __builtin_amdgcn_raw_buffer_load_b128(rs, aoff, 0, AUX_SC1_VOL);
  u4 av1 = __builtin_amdgcn_raw_buffer_load_b128(rs, aoff + 16, 0, AUX_SC1_VOL);
#pragma unroll
  for (int s = 1; s < 4; ++s)
#pragma unroll
    for (int j = 0; j < 4; ++j) *(f4*)(sm + ((((w + s) & 3) * 3 + (3 - s)) * 4 + j) * 1024 + l * 16) = acc[j][s];
  lbar();
  f4 v[4];
#pragma unroll
  for (int j = 0; j < 4; ++j) {
    v[j] = acc[j][0];
#pragma unroll
    for (int sI = 0; sI < 3; ++sI) v[j] += *(const f4*)(sm + ((w * 3 + sI) * 4 + j) * 1024 + l * 16);
    PIN(v[j]);
  }
  for (unsigned spins = 0;; ++spins) {
    const bool miss = (av0.x == 0xffffffffu) | (av0.y == 0xffffffffu) | (av0.z == 0xffffffffu) | (av0.w == 0xffffffffu) |
                      (av1.x == 0xffffffffu) | (av1.y == 0xffffffffu) | (av1.z == 0xffffffffu) | (av1.w == 0xffffffffu);
    if (!__any(miss) || spins > (1u << 16)) break;
    if (spins == 0) {
      GAS unsigned* c = (GAS unsigned*)(cnt + (pn * 8 + (pm & 7)) * CNT_STR);
      for (unsigned sp = 0; sp < (1u << 18); ++sp) {
        const unsigned cv = __builtin_amdgcn_readfirstlane(__hip_atomic_load(c, __ATOMIC_RELAXED, __HIP_MEMORY_SCOPE_AGENT));
        if (cv >= 64u) break;
        __builtin_amdgcn_s_sleep(8);
      }
    } else {
      __builtin_amdgcn_s_sleep(2);
    }
    if (miss) {
      av0 = __builtin_amdgcn_raw_buffer_load_b128(rs, aoff, 0, AUX_SC1_VOL);
      av1 = __builtin_amdgcn_raw_buffer_load_b128(rs, aoff + 16, 0, AUX_SC1_VOL);
    }
  }
  asm volatile("s_waitcnt lgkmcnt(0)" ::: "memory");
  char* myR = sm + w * 12288;
  *(u4*)(myR + l * 32) = av0;
  *(u4*)(myR + l * 32 + 16) = av1;
  asm volatile("s_waitcnt lgkmcnt(0)" ::: "memory");
  u2 ov[4];
#pragma unroll
  for (int j = 0; j < 4; ++j) {
    const b4 a4 = trread(myR + (16 * j + 4 * g + q) * 32 + 8 * p);
    const u2 xv = xr[j];
    const float xs[4] = {lo_bf(xv.x), hi_bf(xv.x), lo_bf(xv.y), hi_bf(xv.y)};
    float o[4];
#pragma unroll
    for (int r = 0; r < 4; ++r) o[r] = bfr(xs[r] + (float)a4[r]) + bfr(v[j][r]);
    ov[j] = (u2){pk_bf(o[0], o[1]), pk_bf(o[2], o[3])};
  }
  const unsigned oo = ((t0 + 16 * w + li) * SD + d0 + 16 * (g & 1) + 8 * (g >> 1)) * 2;
  st16(out, SL * SD * 2, oo, pair16(ov[0], ov[1]));
  st16(out, SL * SD * 2, oo + 64, pair16(ov[2], ov[3]));
}

constexpr int P1_LDS = G1_LDS > KG_LDS ? G1_LDS : KG_LDS;
constexpr int P2_LDS = G2_LDS > CV_LDS ? G2_LDS : CV_LDS;

__global__ __launch_bounds__(512, 4) void k_phase1(const u16* x, const u16* Are, const u16* Aim, const u16* Bre,
                                                   const u16* Bim, const u16* Cre, const u16* Cim, const u16* logdt,
                                                   const u16* w1, const u16* w2, char* ws) {
  extern __shared__ __attribute__((aligned(16))) char sm[];
  const int bid = blockIdx.x;
  if (bid >= 256) {
    if (bid == 256 && threadIdx.x < 64) ((unsigned*)(ws + WS_CNT))[threadIdx.x * CNT_STR] = 0u;
    g1_role(bid - 256, sm, x, w1, (u16*)(ws + WS_H), (_Float16*)(ws + WS_XNT));
  } else {
    kg_role(bid, sm, Are, Aim, Bre, Bim, Cre, Cim, logdt, w2, (_Float16*)(ws + WS_KREV), (float*)(ws + WS_KSC),
            (u16*)(ws + WS_W2F), (u16*)(ws + WS_AT));
  }
}

__global__ __launch_bounds__(256, 3) void k_phase2(const u16* x, const u16* dskip, char* ws, u16* out) {
  extern __shared__ __attribute__((aligned(16))) char sm[];
  const int bid = blockIdx.x;
  if (bid >= 512) {
    g2_role(bid - 512, sm, x, (const u16*)(ws + WS_H), (const u16*)(ws + WS_W2F), (u16*)(ws + WS_AT), (unsigned*)(ws + WS_CNT), out);
  } else {
    cv_role(bid, sm, (const _Float16*)(ws + WS_XNT), (const _Float16*)(ws + WS_KREV), (const float*)(ws + WS_KSC),
            dskip, (u16*)(ws + WS_AT), (unsigned*)(ws + WS_CNT));
  }
}

static void launch_p1(void* const* d_in, void* d_ws, hipStream_t stream) {
  (void)hipFuncSetAttribute((const void*)k_phase1, hipFuncAttributeMaxDynamicSharedMemorySize, P1_LDS);
  k_phase1<<<512, 512, P1_LDS, stream>>>((const u16*)d_in[0], (const u16*)d_in[1], (const u16*)d_in[2], (const u16*)d_in[3],
                                          (const u16*)d_in[4], (const u16*)d_in[5], (const u16*)d_in[6],
                                          (const u16*)d_in[7], (const u16*)d_in[10], (const u16*)d_in[11], (char*)d_ws);
}
static void launch_p2(void* const* d_in, void* d_out, void* d_ws, hipStream_t stream) {
  (void)hipFuncSetAttribute((const void*)k_phase2, hipFuncAttributeMaxDynamicSharedMemorySize, P2_LDS);
  k_phase2<<<768, 256, P2_LDS, stream>>>((const u16*)d_in[0], (const u16*)d_in[8], (char*)d_ws, (u16*)d_out);
}

extern "C" void kernel_launch(void* const* d_in, const int* in_sizes, int n_in, void* d_out, int out_size,
                              void* d_ws, size_t ws_size, hipStream_t stream) {
  launch_p1(d_in, d_ws, stream);
  launch_p2(d_in, d_out, d_ws, stream);
}
```
